```python
import torch

def _softmax(x, axis=-1):
    return torch.softmax(x, dim=axis)
def _gelu(x):
    return torch.nn.functional.gelu(x, approximate="tanh")
def _silu(x):
    return torch.nn.functional.silu(x)
def _sigmoid(x):
    return torch.sigmoid(x)
def _layernorm(x, axis=-1, eps=1e-5):
    m = x.mean(dim=axis, keepdim=True)
    v = x.var(dim=axis, keepdim=True, unbiased=False)
    return (x - m) / torch.sqrt(v + eps)
def _scatter_add(tgt, idx, src, axis=0):
    return tgt.clone().index_add_(axis, idx, src)
def _segment_sum(src, seg, n):
    out = torch.zeros((n,) + src.shape[1:], dtype=src.dtype)
    return out.index_add_(0, seg, src)
def _pad(x, pw):
    flat = tuple(v for pair in reversed(pw) for v in pair)
    return torch.nn.functional.pad(x, flat)

kernel_name = 'gated_mix__mha_block__sliding_block__prompt'

def setup_inputs():

    _w = (lambda a: (lambda t: t.to(torch.bfloat16) if t.is_floating_point() else t)(torch.as_tensor(a)))
    def _A_setup():
        B, S, D, H = 1, 2048, 640, 32
        return {
            "x": _w((lambda *s: torch.randn(*s, dtype=torch.bfloat16) * 0.05)(B, S, D)),
            "wqkv": _w((lambda *s: torch.randn(*s, dtype=torch.bfloat16) * 3.2)(D, 3 * D)),
            "wo": _w((lambda *s: torch.randn(*s, dtype=torch.bfloat16) * 0.1)(D, D)),
        }
    _A_in = _A_setup()

    def _B_setup():
        B, S, D = 1, 2048, 640
        return {
            "x": _w((lambda *s: torch.randn(*s, dtype=torch.bfloat16) * 0.05)(B, S, D)),
            "wqkv": _w((lambda *s: torch.randn(*s, dtype=torch.bfloat16) * 3.2)(D, 3 * D)),
            "wo": _w((lambda *s: torch.randn(*s, dtype=torch.bfloat16) * 0.1)(D, D)),
        }
    _B_in = _B_setup()

    return {
        "A_x": _A_in["x"],
        "A_wqkv": _A_in["wqkv"],
        "A_wo": _A_in["wo"],
        "B_x": _B_in["x"],
        "B_wqkv": _B_in["wqkv"],
        "B_wo": _B_in["wo"],
        "gate_w": _w((lambda *s: torch.randn(*s, dtype=torch.bfloat16) * 0.05)(640) * 16.0),
    }

def reference(A_x, A_wqkv, A_wo, B_x, B_wqkv, B_wo, gate_w):
    def _A_ref(x, wqkv, wo):
        B, S, D = x.shape
        H = 32
        Dh = D // H
        qkv = (lambda x, *d: x.permute(*d))((x @ wqkv).reshape(B, S, 3, H, Dh), 2, 0, 3, 1, 4)
        q, k, v = qkv[0], qkv[1], qkv[2]
        scores = torch.einsum("bhqd,bhkd->bhqk", q, k) * (Dh ** -0.5)
        p = _softmax(scores, axis=-1)
        o = torch.einsum("bhqk,bhkd->bhqd", p, v)
        return (lambda x, *d: x.permute(*d))(o, 0, 2, 1, 3).reshape(B, S, D) @ wo

    def _B_ref(x, wqkv, wo):
        B, S, D = x.shape
        H = 32; Dh = D // H; W = 256
        qkv = (lambda x, *d: x.permute(*d))((x @ wqkv).reshape(B, S, 3, H, Dh), 2, 0, 3, 1, 4)
        q, k, v = qkv[0], qkv[1], qkv[2]
        scores = torch.einsum("bhqd,bhkd->bhqk", q, k) * (Dh ** -0.5)
        idx = torch.arange(S)
        mask = (idx[:, None] - idx[None, :] >= 0) & (idx[:, None] - idx[None, :] < W)
        scores = torch.where(mask[None, None], scores, -1e9)
        p = _softmax(scores, axis=-1)
        o = torch.einsum("bhqk,bhkd->bhqd", p, v)
        return (lambda x, *d: x.permute(*d))(o, 0, 2, 1, 3).reshape(B, S, D) @ wo

    x = A_x
    g = _sigmoid((x * gate_w).sum(-1, keepdims=True))
    a = _A_ref(x, A_wqkv, A_wo)
    b = _B_ref(x, B_wqkv, B_wo)
    return g * a + (1 - g) * b
```

```cpp
#include <hip/hip_runtime.h>
#include <hip/hip_bf16.h>
#define MIMIC 0
typedef __hip_bfloat16 bf16;
__device__ __forceinline__ float rb(float x){ return __bfloat162float(__float2bfloat16(x)); }
__global__ void k_qkv(const bf16* x, const bf16* wa, const bf16* wb, bf16* qkv){
  int n = blockIdx.x*256+threadIdx.x;
  int s = blockIdx.y;
  const bf16* w = n<1920? wa: wb; int nn = n<1920? n: n-1920;
  float acc=0;
  for(int k=0;k<640;k++) acc += __bfloat162float(x[s*640+k])*__bfloat162float(w[k*1920+nn]);
  qkv[(size_t)(n<1920?0:1)*2048*1920 + (size_t)s*1920+nn] = __float2bfloat16(acc);
}
__global__ __launch_bounds__(64) void k_attn(const bf16* qkv, bf16* o){
  int q = blockIdx.x*64+threadIdx.x; int h = blockIdx.y; int br = blockIdx.z;
  const bf16* base = qkv + (size_t)br*2048*1920;
  float qv[20]; _Pragma("unroll") for(int d=0;d<20;d++) qv[d]=__bfloat162float(base[(size_t)q*1920+h*20+d]);
  int k0 = br? max(0,q-255):0, k1 = br? q+1:2048;
  const float scale = 0.22360679774997896f;
  float m=-1e30f;
  _Pragma("unroll 1") for(int k=k0;k<k1;k++){ float s=0; _Pragma("unroll") for(int d=0;d<20;d++) s+=qv[d]*__bfloat162float(base[(size_t)k*1920+640+h*20+d]);
    if(MIMIC){ s=rb(rb(s)*scale);} else s*=scale; m=fmaxf(m,s);}
  float l=0;
  _Pragma("unroll 1") for(int k=k0;k<k1;k++){ float s=0; _Pragma("unroll") for(int d=0;d<20;d++) s+=qv[d]*__bfloat162float(base[(size_t)k*1920+640+h*20+d]);
    if(MIMIC){ s=rb(rb(s)*scale);} else s*=scale; l+=expf(s-m);}
  float acc[20]; _Pragma("unroll") for(int d=0;d<20;d++) acc[d]=0;
  _Pragma("unroll 1") for(int k=k0;k<k1;k++){ float s=0; _Pragma("unroll") for(int d=0;d<20;d++) s+=qv[d]*__bfloat162float(base[(size_t)k*1920+640+h*20+d]);
    if(MIMIC){ s=rb(rb(s)*scale);} else s*=scale; float p=expf(s-m)/l; if(MIMIC) p=rb(p);
    _Pragma("unroll") for(int d=0;d<20;d++) acc[d]+=p*__bfloat162float(base[(size_t)k*1920+1280+h*20+d]); }
  _Pragma("unroll") for(int d=0;d<20;d++) o[(size_t)br*2048*640+(size_t)q*640+h*20+d]=__float2bfloat16(acc[d]);
}
__global__ void k_out(const bf16* x, const bf16* gw, const bf16* o, const bf16* woa, const bf16* wob, bf16* out){
  int s = blockIdx.x; int n = threadIdx.x;
  float gs=0; for(int k=0;k<640;k++) gs += rb(__bfloat162float(x[s*640+k])*__bfloat162float(gw[k]));
  gs = rb(gs); float g = rb(1.f/(1.f+expf(-gs)));
  float a=0,b=0;
  for(int k=0;k<640;k++){ a+=__bfloat162float(o[(size_t)s*640+k])*__bfloat162float(woa[k*640+n]); b+=__bfloat162float(o[(size_t)2048*640+(size_t)s*640+k])*__bfloat162float(wob[k*640+n]); }
  a=rb(a); b=rb(b);
  float r = rb(g*a)+rb(rb(1.f-g)*b);
  out[(size_t)s*640+n]=__float2bfloat16(r);
}
extern "C" void kernel_launch(void* const* d_in, const int* in_sizes, int n_in, void* d_out, int out_size, void* d_ws, size_t ws_size, hipStream_t stream){
  const bf16* x=(const bf16*)d_in[0]; const bf16* wa=(const bf16*)d_in[1]; const bf16* woa=(const bf16*)d_in[2];
  const bf16* wb=(const bf16*)d_in[4]; const bf16* wob=(const bf16*)d_in[5]; const bf16* gw=(const bf16*)d_in[6];
  bf16* qkv=(bf16*)d_ws; bf16* o = qkv + (size_t)2*2048*1920;
  k_qkv<<<dim3(15,2048),256,0,stream>>>(x,wa,wb,qkv);
  k_attn<<<dim3(32,32,2),64,0,stream>>>(qkv,o);
  k_out<<<2048,640,0,stream>>>(x,gw,o,woa,wob,(bf16*)d_out);
}
```

```cpp
#include <hip/hip_runtime.h>
#include <hip/hip_bf16.h>
#include <stdint.h>

typedef __attribute__((ext_vector_type(8))) short bf16x8;
typedef __attribute__((ext_vector_type(4))) short bf16x4;
typedef __attribute__((ext_vector_type(16))) float f32x16;
typedef __attribute__((ext_vector_type(4))) float f32x4;
typedef __attribute__((ext_vector_type(4))) unsigned u32x4;
typedef __attribute__((ext_vector_type(2))) unsigned u32x2;
typedef short v4i16_t __attribute__((ext_vector_type(4)));
typedef __attribute__((address_space(3))) const char* lds_cptr;
typedef unsigned short u16;

#define DI __device__ __forceinline__
#define MFMA32(a, b, c) __builtin_amdgcn_mfma_f32_32x32x16_bf16((a), (b), (c), 0, 0, 0)
#define MFMA32K8(a, b, c) __builtin_amdgcn_mfma_f32_32x32x8bf16_1k((a), (b), (c), 0, 0, 0)
#define MFMA16(a, b, c) __builtin_amdgcn_mfma_f32_16x16x32_bf16((a), (b), (c), 0, 0, 0)

constexpr int SEQ = 2048, DM = 640, NH = 32, DH = 20;
constexpr int TILE_B = 3328;
constexpr int CHUNK_B = 8 * TILE_B;
constexpr int QT_B = 1280;
constexpr size_t WS_KV = 0;
constexpr size_t WS_Q = WS_KV + (size_t)2 * 32 * 64 * TILE_B;
constexpr size_t WS_O = WS_Q + (size_t)2 * 32 * 64 * QT_B;
constexpr size_t WS_G = WS_O + (size_t)2 * SEQ * DM * 2;

DI unsigned cvtpk(float lo, float hi) { unsigned r; asm("v_cvt_pk_bf16_f32 %0, %1, %2" : "=v"(r) : "v"(lo), "v"(hi)); return r; }
DI float bf2f(u16 v) { return __uint_as_float(((unsigned)v) << 16); }
DI float rbf(float x) { return __uint_as_float(cvtpk(x, x) << 16); }

DI void glds16(const void* g, unsigned lds_base) {
    unsigned sv; asm volatile("s_mov_b32 %0, m0\n\ts_mov_b32 m0, %2\n\ts_nop 0\n\tglobal_load_lds_dwordx4 %1, off\n\ts_mov_b32 m0, %0" : "=&s"(sv) : "v"(g), "s"(lds_base) : "memory"); }
DI void glds4(const void* g, unsigned lds_base) {
    unsigned sv; asm volatile("s_mov_b32 %0, m0\n\ts_mov_b32 m0, %2\n\ts_nop 0\n\tglobal_load_lds_dword %1, off\n\ts_mov_b32 m0, %0" : "=&s"(sv) : "v"(g), "s"(lds_base) : "memory"); }
DI bf16x4 trread(lds_cptr p) { return __builtin_bit_cast(bf16x4, __builtin_amdgcn_ds_read_tr16_b64_v4i16((__attribute__((address_space(3))) v4i16_t*)p)); }
DI bf16x8 ldsr128(lds_cptr p) { return *(const __attribute__((address_space(3))) bf16x8*)p; }
DI bf16x4 ldsr64(lds_cptr p) { return *(const __attribute__((address_space(3))) bf16x4*)p; }
#define WAIT_BAR0() asm volatile("s_waitcnt vmcnt(0) lgkmcnt(0)\n\ts_barrier" ::: "memory")

constexpr int K1_XB = 256 * 128;
constexpr int K1_WB = 64 * 256;

template <bool VT>
DI void k1_body(const u16* __restrict__ X, const u16* __restrict__ W, char* __restrict__ ws, char* lds, int m0, int n0, int branch, int type) {
    const int tid = threadIdx.x, lane = tid & 63, hh = lane >> 5, l31 = lane & 31;
    const int wid = __builtin_amdgcn_readfirstlane(tid >> 6);
    const int wm = wid >> 1, wn = wid & 1;
    const unsigned lds0 = (unsigned)(uintptr_t)lds;
    const u16* xs[4];
#pragma unroll
    for (int i = 0; i < 4; ++i) { const int row = 32 * wid + 8 * i + (lane >> 3); const int c = (lane & 7) ^ ((row >> 1) & 7); xs[i] = X + (size_t)(m0 + row) * DM + c * 8; }
    const u16* wsrc[2];
#pragma unroll
    for (int j = 0; j < 2; ++j) { const int kr = 8 * wid + 4 * j + (lane >> 4); const int c = (lane & 15) ^ ((kr & 3) << 2); wsrc[j] = W + (size_t)kr * 1920 + n0 + c * 8; }
    auto stage = [&](int kt, int buf) {
        const unsigned xb = lds0 + buf * K1_XB + wid * 4096, wb = lds0 + 3 * K1_XB + buf * K1_WB + wid * 2048;
#pragma unroll
        for (int i = 0; i < 4; ++i) glds16(xs[i] + kt * 64, xb + i * 1024);
#pragma unroll
        for (int j = 0; j < 2; ++j) glds16(wsrc[j] + (size_t)kt * 64 * 1920, wb + j * 1024);
    };
    const int sw = (l31 >> 1) & 7;
    int xoff[4];
#pragma unroll
    for (int ks = 0; ks < 4; ++ks) xoff[ks] = (wm * 64 + l31) * 128 + (((2 * ks + hh) ^ sw) * 16);
    const int q = (lane & 15) >> 2, p = lane & 3, ch = (lane >> 4) & 1;
    int woff[2];
#pragma unroll
    for (int ni = 0; ni < 2; ++ni) woff[ni] = (8 * hh + q) * 256 + (((wn * 2 + ni) ^ q) * 64) + ch * 32 + p * 8;

    f32x16 acc[2][2];
#pragma unroll
    for (int a = 0; a < 2; ++a)
#pragma unroll
        for (int b = 0; b < 2; ++b) acc[a][b] = f32x16{};

    stage(0, 0); stage(1, 1);
    int buf = 0;
    for (int kt = 0; kt < 10; ++kt) {
        if (kt < 9) asm volatile("s_waitcnt vmcnt(6) lgkmcnt(0)\n\ts_barrier" ::: "memory"); else WAIT_BAR0();
        const int nb = (buf == 0) ? 2 : buf - 1;
        if (kt + 2 < 10) stage(kt + 2, nb);
        const lds_cptr xb = (lds_cptr)lds + buf * K1_XB, wb = (lds_cptr)lds + 3 * K1_XB + buf * K1_WB;
        buf = (buf == 2) ? 0 : buf + 1;
#pragma unroll
        for (int ks = 0; ks < 4; ++ks) {
            bf16x8 xf[2], wf[2];
#pragma unroll
            for (int mi = 0; mi < 2; ++mi) xf[mi] = ldsr128(xb + xoff[ks] + mi * 4096);
#pragma unroll
            for (int ni = 0; ni < 2; ++ni) {
                const bf16x4 lo = trread(wb + woff[ni] + ks * 4096), hi = trread(wb + woff[ni] + ks * 4096 + 1024);
                wf[ni] = __builtin_shufflevector(lo, hi, 0, 1, 2, 3, 4, 5, 6, 7);
            }
#pragma unroll
            for (int mi = 0; mi < 2; ++mi)
#pragma unroll
                for (int ni = 0; ni < 2; ++ni)
                    acc[mi][ni] = VT ? MFMA32(xf[mi], wf[ni], acc[mi][ni]) : MFMA32(wf[ni], xf[mi], acc[mi][ni]);
        }
    }
    const int fb0 = (n0 - type * 640) + wn * 64;
    if (VT) {
        char* kv = ws + WS_KV + (size_t)branch * 32 * 64 * TILE_B;
#pragma unroll
        for (int ni = 0; ni < 2; ++ni) {
            const int f = fb0 + ni * 32 + l31, head = f / 20, d = f - head * 20;
#pragma unroll
            for (int mi = 0; mi < 2; ++mi) {
                const int kt = (m0 >> 5) + wm * 2 + mi;
                char* dst = kv + ((size_t)head * 64 + kt) * TILE_B + 1280 + hh * 512 + d * 16;
#pragma unroll
                for (int s = 0; s < 2; ++s) {
                    const f32x16& a = acc[mi][ni];
                    u32x4 v = {cvtpk(a[8 * s], a[8 * s + 1]), cvtpk(a[8 * s + 2], a[8 * s + 3]), cvtpk(a[8 * s + 4], a[8 * s + 5]), cvtpk(a[8 * s + 6], a[8 * s + 7])};
                    *(u32x4*)(dst + s * 1024) = v;
                    if (d == 19) *(u32x4*)(dst + s * 1024 + 16) = (u32x4){0x3f803f80u, 0x3f803f80u, 0x3f803f80u, 0x3f803f80u};
                }
            }
        }
    } else {
        const float sc = (type == 0) ? 0.32259857558f : 1.0f;
        char* base = (type == 0) ? (ws + WS_Q + (size_t)branch * 32 * 64 * QT_B) : (ws + WS_KV + (size_t)branch * 32 * 64 * TILE_B);
        const int rec = (type == 0) ? QT_B : TILE_B;
#pragma unroll
        for (int ni = 0; ni < 2; ++ni)
#pragma unroll
            for (int g = 0; g < 4; ++g) {
                const int f0 = fb0 + ni * 32 + 8 * g + 4 * hh, head = f0 / 20, d0 = f0 - head * 20;
                const int off = (d0 < 16) ? ((d0 >> 3) * 512 + l31 * 16 + (d0 & 7) * 2) : (1024 + l31 * 8);
#pragma unroll
                for (int mi = 0; mi < 2; ++mi) {
                    const int tt = (m0 >> 5) + wm * 2 + mi;
                    const f32x16& a = acc[mi][ni];
                    u32x2 v = {cvtpk(a[4 * g] * sc, a[4 * g + 1] * sc), cvtpk(a[4 * g + 2] * sc, a[4 * g + 3] * sc)};
                    *(u32x2*)(base + ((size_t)head * 64 + tt) * rec + off) = v;
                }
            }
    }
}

__global__ __launch_bounds__(512, 2) void k1_qkv(const u16* __restrict__ X, const u16* __restrict__ Wa, const u16* __restrict__ Wb, const u16* __restrict__ gw, char* __restrict__ ws) {
    __shared__ __attribute__((aligned(16))) char lds[3 * K1_XB + 3 * K1_WB];
    const int bid = blockIdx.x;
    if (bid >= 240) {
        const int tok = (bid - 240) * 128 + (threadIdx.x >> 2), part = threadIdx.x & 3;
        const u16* xr = X + (size_t)tok * DM + part * 160; const u16* gr = gw + part * 160;
        float s = 0.f;
#pragma unroll 4
        for (int i = 0; i < 20; ++i) {
            const bf16x8 xv = *(const bf16x8*)(xr + i * 8), gv = *(const bf16x8*)(gr + i * 8);
#pragma unroll
            for (int j = 0; j < 8; ++j) s += rbf(bf2f((u16)xv[j]) * bf2f((u16)gv[j]));
        }
        s += __shfl_xor(s, 1); s += __shfl_xor(s, 2);
        if (part == 0) { const float z = rbf(s); ((float*)(ws + WS_G))[tok] = rbf(1.0f / (1.0f + __expf(-z))); }
        return;
    }
    const int xcd = bid & 7, idx = bid >> 3;
    const int branch = xcd >> 2, mt = (xcd & 3) * 2 + (idx / 15), nt = idx % 15, type = nt / 5;
    const u16* W = branch ? Wb : Wa;
    if (type == 2) k1_body<true>(X, W, ws, lds, mt * 256, nt * 128, branch, type);
    else k1_body<false>(X, W, ws, lds, mt * 256, nt * 128, branch, type);
}

constexpr int K2_LDS = 5 * CHUNK_B + 64;
constexpr float REF_OFF = 90.0f;
#define SB() __builtin_amdgcn_sched_barrier(0)
#define PIN(x) asm volatile("" : "+v"(x))
#define EX(v) __builtin_amdgcn_exp2f(v)

DI void stage_chunk(const char* src, unsigned ldsbase, int wid, int lane) {
    const char* s = src + wid * 3072 + lane * 16;
    const unsigned d = ldsbase + wid * 3072;
    glds16(s, d); glds16(s + 1024, d + 1024); glds16(s + 2048, d + 2048);
    glds4(src + 24576 + wid * 256 + lane * 4, ldsbase + 24576 + wid * 256);
}
DI float fmax16(const f32x16& s) {
    float a = fmaxf(fmaxf(s[0], s[1]), s[2]), b = fmaxf(fmaxf(s[3], s[4]), s[5]);
    a = fmaxf(fmaxf(a, s[6]), s[7]); b = fmaxf(fmaxf(b, s[8]), s[9]);
    a = fmaxf(fmaxf(a, s[10]), s[11]); b = fmaxf(fmaxf(b, s[12]), s[13]);
    a = fmaxf(fmaxf(a, s[14]), s[15]);
    return fmaxf(a, b);
}
DI f32x16 splat16(float v) { f32x16 r;
#pragma unroll
    for (int i = 0; i < 16; ++i) r[i] = v; return r; }
template <bool NEXT>
DI void phase(f32x16& X, f32x16& o, const bf16x8& vf0, const bf16x8& vf1, f32x16& Y, const bf16x8& kf0, const bf16x4& kf1, const bf16x8& qy0, const bf16x4& qy1, const f32x16& nmy) {
    SB();
    X[0] = EX(X[0]); X[1] = EX(X[1]); X[2] = EX(X[2]); X[3] = EX(X[3]); PIN(X); SB();
    if (NEXT) { Y = MFMA32(kf0, qy0, nmy); PIN(Y); SB(); }
    X[4] = EX(X[4]); X[5] = EX(X[5]); X[6] = EX(X[6]); X[7] = EX(X[7]); PIN(X); SB();
    if (NEXT) { Y = MFMA32K8(kf1, qy1, Y); PIN(Y); SB(); }
    u32x4 p0 = {cvtpk(X[0], X[1]), cvtpk(X[2], X[3]), cvtpk(X[4], X[5]), cvtpk(X[6], X[7])}; PIN(p0);
    X[8] = EX(X[8]); X[9] = EX(X[9]); X[10] = EX(X[10]); X[11] = EX(X[11]); PIN(X); SB();
    o = MFMA32(vf0, __builtin_bit_cast(bf16x8, p0), o); PIN(o); SB();
    X[12] = EX(X[12]); X[13] = EX(X[13]); X[14] = EX(X[14]); X[15] = EX(X[15]);
    u32x4 p1 = {cvtpk(X[8], X[9]), cvtpk(X[10], X[11]), cvtpk(X[12], X[13]), cvtpk(X[14], X[15])}; PIN(p1); SB();
    o = MFMA32(vf1, __builtin_bit_cast(bf16x8, p1), o); PIN(o); SB();
}
DI bool bad_f(float x) { return !(fabsf(x) <= 3.0e38f); }
DI void store_o(const f32x16& o, char* ws, int branch, int head, int qtile, int lane) {
    const int hh = lane >> 5, l31 = lane & 31;
    float l = o[8];
    l = __shfl(l, 32 + l31);
    const float rl = 1.0f / l;
    const int tok = qtile * 32 + l31;
    char* base = ws + WS_O + ((size_t)(branch * 128 + (tok >> 4)) * 20) * 1024 + (tok & 15) * 16;
#pragma unroll
    for (int g = 0; g < 3; ++g) {
        if (g == 2 && hh) break;
        const int k0 = head * 20 + 8 * g + 4 * hh;
        const int ks = k0 >> 5, kk = k0 & 31;
        u32x2 v = {cvtpk(o[4 * g] * rl, o[4 * g + 1] * rl), cvtpk(o[4 * g + 2] * rl, o[4 * g + 3] * rl)};
        *(u32x2*)(base + ks * 1024 + ((kk & 15) >> 2) * 256 + (kk >> 4) * 8) = v;
    }
}

__global__ __launch_bounds__(512, 2) void k2_attn(char* __restrict__ ws) {
    __shared__ __attribute__((aligned(16))) char lds[K2_LDS];
    const int tid = threadIdx.x, lane = tid & 63, hh = lane >> 5, l31 = lane & 31;
    const int wid = __builtin_amdgcn_readfirstlane(tid >> 6);
    const int bid = blockIdx.x;
    const int head = (bid & 7) * 4 + ((bid >> 3) & 3), qb = bid >> 5;
    const unsigned lds0 = (unsigned)(uintptr_t)lds;
    const char* kvA = ws + WS_KV + (size_t)head * 64 * TILE_B;
    const char* kvB = ws + WS_KV + (size_t)(32 + head) * 64 * TILE_B;
    volatile int* flag = (volatile int*)(lds + 5 * CHUNK_B);
    const int pr = wid >> 1, kh = wid & 1;
    const lds_cptr l16 = (lds_cptr)lds + lane * 16, l8 = (lds_cptr)lds + 1024 + l31 * 8;

    stage_chunk(kvA, lds0, wid, lane);
    bf16x8 qa0, qb0; bf16x4 qa1, qb1;
    {
        const char* qp = ws + WS_Q + ((size_t)head * 64 + qb * 8 + 2 * pr) * QT_B;
        qa0 = *(const bf16x8*)(qp + lane * 16); qb0 = *(const bf16x8*)(qp + QT_B + lane * 16);
        const bf16x4 ta = *(const bf16x4*)(qp + 1024 + l31 * 8), tb = *(const bf16x4*)(qp + QT_B + 1024 + l31 * 8);
        qa1 = hh ? bf16x4{0, 0, 0, 0} : ta; qb1 = hh ? bf16x4{0, 0, 0, 0} : tb;
    }
    if (tid == 0) *flag = 0;
    WAIT_BAR0();
    stage_chunk(kvA + CHUNK_B, lds0 + CHUNK_B, wid, lane);
    if (qb > 0) stage_chunk(kvB + (size_t)(qb - 1) * CHUNK_B, lds0 + 3 * CHUNK_B, wid, lane);
    stage_chunk(kvB + (size_t)qb * CHUNK_B, lds0 + 4 * CHUNK_B, wid, lane);

    float ma, mb;
    {
        const bf16x8 kf0 = ldsr128(l16); const bf16x4 kf1 = ldsr64(l8);
        f32x16 s = MFMA32(kf0, qa0, f32x16{}); s = MFMA32K8(kf1, qa1, s);
        f32x16 u = MFMA32(kf0, qb0, f32x16{}); u = MFMA32K8(kf1, qb1, u);
        ma = fmax16(s); ma = fmaxf(ma, __shfl_xor(ma, 32)) + REF_OFF;
        mb = fmax16(u); mb = fmaxf(mb, __shfl_xor(mb, 32)) + REF_OFF;
    }
    f32x16 keep;
    for (int attempt = 0; attempt < 2; ++attempt) {
        if (attempt == 1) {
            ma = -3.0e38f; mb = -3.0e38f;
            stage_chunk(kvA, lds0, wid, lane);
            WAIT_BAR0();
            for (int c = 0; c < 8; ++c) {
                if (c < 7) stage_chunk(kvA + (size_t)(c + 1) * CHUNK_B, lds0 + ((c + 1) & 1) * CHUNK_B, wid, lane);
                for (int t = 0; t < 8; ++t) {
                    const int off = (c & 1) * CHUNK_B + t * TILE_B;
                    const bf16x8 kf0 = ldsr128(l16 + off); const bf16x4 kf1 = ldsr64(l8 + off);
                    f32x16 s = MFMA32(kf0, qa0, f32x16{}); s = MFMA32K8(kf1, qa1, s);
                    f32x16 u = MFMA32(kf0, qb0, f32x16{}); u = MFMA32K8(kf1, qb1, u);
                    ma = fmaxf(ma, fmax16(s)); mb = fmaxf(mb, fmax16(u));
                }
                WAIT_BAR0();
            }
            ma = fmaxf(ma, __shfl_xor(ma, 32)); mb = fmaxf(mb, __shfl_xor(mb, 32));
            stage_chunk(kvA, lds0, wid, lane);
            WAIT_BAR0();
            stage_chunk(kvA + CHUNK_B, lds0 + CHUNK_B, wid, lane);
        }
        const f32x16 nma = splat16(-ma), nmb = splat16(-mb);
        f32x16 o0 = f32x16{}, o1 = f32x16{}, S0, S1;
        int cur = kh * 4 * TILE_B;
        bf16x8 kf0 = ldsr128(l16 + cur), vf0 = ldsr128(l16 + cur + 1280), vf1 = ldsr128(l16 + cur + 2304); bf16x4 kf1 = ldsr64(l8 + cur);
        S0 = MFMA32(kf0, qa0, nma); S0 = MFMA32K8(kf1, qa1, S0);
        int slot = 0;
        for (int c = 0; c < 8; ++c) {
            const int nslot = (slot == 2) ? 0 : slot + 1, pslot = (slot == 0) ? 2 : slot - 1;
            const int nxt = nslot * CHUNK_B + kh * 4 * TILE_B;
#pragma unroll
            for (int t = 0; t < 4; ++t) {
                if (t == 3) {
                    asm volatile("s_waitcnt vmcnt(0)\n\ts_barrier" ::: "memory");
                    if (c + 2 < 8) stage_chunk(kvA + (size_t)(c + 2) * CHUNK_B, lds0 + pslot * CHUNK_B, wid, lane);
                }
                const int no = (t < 3) ? cur + (t + 1) * TILE_B : nxt;
                const bf16x8 nk0 = ldsr128(l16 + no); const bf16x4 nk1 = ldsr64(l8 + no);
                phase<true>(S0, o0, vf0, vf1, S1, kf0, kf1, qb0, qb1, nmb);
                const bf16x8 nv0 = ldsr128(l16 + no + 1280), nv1 = ldsr128(l16 + no + 2304);
                phase<true>(S1, o1, vf0, vf1, S0, nk0, nk1, qa0, qa1, nma);
                kf0 = nk0; kf1 = nk1; vf0 = nv0; vf1 = nv1;
            }
            cur = nxt; slot = nslot;
        }
        {
            float* ex = (float*)lds + wid * 12 * 64;
#pragma unroll
            for (int r = 0; r < 12; ++r) ex[r * 64 + lane] = kh ? o0[r] : o1[r];
            __syncthreads();
            const float* in = (const float*)lds + (wid ^ 1) * 12 * 64;
            bool bad = false;
#pragma unroll
            for (int r = 0; r < 12; ++r) { keep[r] = (kh ? o1[r] : o0[r]) + in[r * 64 + lane]; if (r < 9 || !hh) bad |= bad_f(keep[r]); }
            if (attempt == 0 && bad) *flag = 1;
            __syncthreads();
        }
        if (attempt == 1 || *flag == 0) break;
    }
    store_o(keep, ws, 0, head, qb * 8 + 2 * pr + kh, lane);

    {
        const char* qp = ws + WS_Q + ((size_t)(32 + head) * 64 + qb * 8 + wid) * QT_B;
        const bf16x8 q0 = *(const bf16x8*)(qp + lane * 16);
        const bf16x4 tq = *(const bf16x4*)(qp + 1024 + l31 * 8);
        const bf16x4 q1 = hh ? bf16x4{0, 0, 0, 0} : tq;
        const int wb = 3 * CHUNK_B + wid * TILE_B;
        const int i0 = (qb == 0) ? (8 - wid) : 0;
        float m;
        {
            const int off = wb + 8 * TILE_B;
            f32x16 s = MFMA32(ldsr128(l16 + off), q0, f32x16{}); s = MFMA32K8(ldsr64(l8 + off), q1, s);
#pragma unroll
            for (int r = 0; r < 16; ++r) { const int kl = (r & 3) + 8 * (r >> 2) + 4 * hh; if (kl > l31) s[r] = -3.0e38f; }
            m = fmax16(s); m = fmaxf(m, __shfl_xor(m, 32));
        }
        float mr = m + REF_OFF;
        f32x16 ob;
        for (int attempt = 0; attempt < 2; ++attempt) {
            if (attempt == 1) {
                for (int i = i0; i < 8; ++i) {
                    const int off = wb + i * TILE_B;
                    f32x16 s = MFMA32(ldsr128(l16 + off), q0, f32x16{}); s = MFMA32K8(ldsr64(l8 + off), q1, s);
                    if (i == 0) {
#pragma unroll
                        for (int r = 0; r < 16; ++r) { const int kl = (r & 3) + 8 * (r >> 2) + 4 * hh; if (kl <= l31) s[r] = -3.0e38f; }
                    }
                    m = fmaxf(m, fmax16(s));
                }
                mr = fmaxf(m, __shfl_xor(m, 32));
            }
            const f32x16 nm = splat16(-mr);
            ob = f32x16{};
            f32x16 sa, sb;
            int off = wb + i0 * TILE_B;
            bf16x8 vf0 = ldsr128(l16 + off + 1280), vf1 = ldsr128(l16 + off + 2304);
            sa = MFMA32(ldsr128(l16 + off), q0, nm); sa = MFMA32K8(ldsr64(l8 + off), q1, sa);
            if (i0 == 0) {
#pragma unroll
                for (int r = 0; r < 16; ++r) { const int kl = (r & 3) + 8 * (r >> 2) + 4 * hh; if (kl <= l31) sa[r] = -INFINITY; }
            }
            int i = i0;
            bool in_a = true;
            while (i < 8) {
                {   off += TILE_B;
                    const bf16x8 nk0 = ldsr128(l16 + off), nv0 = ldsr128(l16 + off + 1280), nv1 = ldsr128(l16 + off + 2304); const bf16x4 nk1 = ldsr64(l8 + off);
                    phase<true>(sa, ob, vf0, vf1, sb, nk0, nk1, q0, q1, nm);
                    vf0 = nv0; vf1 = nv1; ++i; }
                if (i == 8) { in_a = false; break; }
                {   off += TILE_B;
                    const bf16x8 nk0 = ldsr128(l16 + off), nv0 = ldsr128(l16 + off + 1280), nv1 = ldsr128(l16 + off + 2304); const bf16x4 nk1 = ldsr64(l8 + off);
                    phase<true>(sb, ob, vf0, vf1, sa, nk0, nk1, q0, q1, nm);
                    vf0 = nv0; vf1 = nv1; ++i; }
            }
            if (!in_a) sa = sb;
#pragma unroll
            for (int r = 0; r < 16; ++r) { const int kl = (r & 3) + 8 * (r >> 2) + 4 * hh; if (kl > l31) sa[r] = -INFINITY; }
            phase<false>(sa, ob, vf0, vf1, sb, vf0, q1, q0, q1, nm);
            bool bad = false;
#pragma unroll
            for (int r = 0; r < 12; ++r) if (r < 9 || !hh) bad |= bad_f(ob[r]);
            if (attempt == 1 || !__any(bad)) break;
        }
        store_o(ob, ws, 1, head, qb * 8 + wid, lane);
    }
}

constexpr int K3_CB = 2 * 160 * 160;

__global__ __launch_bounds__(512, 2) void k3_out(const u16* __restrict__ Woa, const u16* __restrict__ Wob, const char* __restrict__ ws, u16* __restrict__ out) {
    __shared__ __attribute__((aligned(16))) char lds[3 * K3_CB];
    const int tid = threadIdx.x, lane = tid & 63;
    const int wid = __builtin_amdgcn_readfirstlane(tid >> 6);
    const int bid = blockIdx.x;
    const int ft = (bid >> 3) & 7, mt = (bid & 7) * 4 + (bid >> 6);
    const int n0 = ft * 80, tt = wid & 3, br = wid >> 2;
    const unsigned lds0 = (unsigned)(uintptr_t)lds;
    const u16* wsrc[7];
#pragma unroll
    for (int i = 0; i < 7; ++i) {
        int u = (wid + 8 * i) * 64 + lane; if (u >= 3200) u = 0;
        const int b = u / 1600, rem = u - b * 1600, row = rem / 10, c = rem - row * 10;
        wsrc[i] = (b ? Wob : Woa) + (size_t)row * DM + n0 + c * 8;
    }
    auto stage = [&](int ck, int buf) {
#pragma unroll
        for (int i = 0; i < 7; ++i) if (wid + 8 * i < 50) glds16(wsrc[i] + (size_t)ck * 160 * DM, lds0 + buf * K3_CB + (wid + 8 * i) * 1024);
    };
    stage(0, 0);
    const int tile16 = mt * 4 + tt;
    const char* op = ws + WS_O + ((size_t)(br * 128 + tile16) * 20) * 1024 + lane * 16;
    bf16x8 of[20];
#pragma unroll
    for (int ks = 0; ks < 20; ++ks) of[ks] = *(const bf16x8*)(op + ks * 1024);
    stage(1, 1); stage(2, 2);
    const int q = (lane & 15) >> 2, p = lane & 3, kq = lane >> 4;
    const int woff = br * 25600 + (4 * kq + q) * 160 + p * 8;
    f32x4 acc[5];
#pragma unroll
    for (int f = 0; f < 5; ++f) acc[f] = f32x4{};
    WAIT_BAR0();
#pragma unroll
    for (int ck = 0; ck < 4; ++ck) {
        const lds_cptr wb = (lds_cptr)lds + (ck % 3) * K3_CB + woff;
#pragma unroll
        for (int ks = 0; ks < 5; ++ks)
#pragma unroll
            for (int f = 0; f < 5; ++f) {
                const bf16x4 lo = trread(wb + ks * 5120 + f * 32), hi = trread(wb + ks * 5120 + 2560 + f * 32);
                acc[f] = MFMA16(__builtin_shufflevector(lo, hi, 0, 1, 2, 3, 4, 5, 6, 7), of[ck * 5 + ks], acc[f]);
            }
        if (ck == 0) { WAIT_BAR0(); stage(3, 0); }
        if (ck == 2) { WAIT_BAR0(); }
    }
    float* ex = (float*)(lds + K3_CB) + tt * 20 * 64;
    if (br == 1) {
#pragma unroll
        for (int f = 0; f < 5; ++f)
#pragma unroll
            for (int r = 0; r < 4; ++r) ex[(f * 4 + r) * 64 + lane] = acc[f][r];
    }
    __syncthreads();
    if (br == 0) {
        const int tok = tile16 * 16 + (lane & 15);
        const float g = ((const float*)(ws + WS_G))[tok], g1 = rbf(1.0f - g);
        u16* orow = out + (size_t)tok * DM + n0 + kq * 4;
#pragma unroll
        for (int f = 0; f < 5; ++f) {
            float v[4];
#pragma unroll
            for (int r = 0; r < 4; ++r) v[r] = rbf(g * rbf(acc[f][r])) + rbf(g1 * rbf(ex[(f * 4 + r) * 64 + lane]));
            *(u32x2*)(orow + f * 16) = (u32x2){cvtpk(v[0], v[1]), cvtpk(v[2], v[3])};
        }
    }
}

extern "C" void kernel_launch(void* const* d_in, const int* in_sizes, int n_in, void* d_out, int out_size, void* d_ws, size_t ws_size, hipStream_t stream) {
    const u16* x = (const u16*)d_in[0];
    const u16* wa = (const u16*)d_in[1];
    const u16* woa = (const u16*)d_in[2];
    const u16* wb = (const u16*)d_in[4];
    const u16* wob = (const u16*)d_in[5];
    const u16* gw = (const u16*)d_in[6];
    char* ws = (char*)d_ws;
    k1_qkv<<<256, 512, 0, stream>>>(x, wa, wb, gw, ws);
    k2_attn<<<256, 512, 0, stream>>>(ws);
    k3_out<<<256, 512, 0, stream>>>(woa, wob, ws, (u16*)d_out);
}
```

```cpp
#include <hip/hip_runtime.h>
#include <hip/hip_bf16.h>
#include <stdint.h>

typedef __attribute__((ext_vector_type(8))) short bf16x8;
typedef __attribute__((ext_vector_type(4))) short bf16x4;
typedef __attribute__((ext_vector_type(16))) float f32x16;
typedef __attribute__((ext_vector_type(4))) float f32x4;
typedef __attribute__((ext_vector_type(4))) unsigned u32x4;
typedef __attribute__((ext_vector_type(2))) unsigned u32x2;
typedef short v4i16_t __attribute__((ext_vector_type(4)));
typedef __attribute__((address_space(3))) const char* lds_cptr;
typedef unsigned short u16;

#define DI __device__ __forceinline__
#define MFMA32(a, b, c) __builtin_amdgcn_mfma_f32_32x32x16_bf16((a), (b), (c), 0, 0, 0)
#define MFMA32K8(a, b, c) __builtin_amdgcn_mfma_f32_32x32x8bf16_1k((a), (b), (c), 0, 0, 0)
#define MFMA16(a, b, c) __builtin_amdgcn_mfma_f32_16x16x32_bf16((a), (b), (c), 0, 0, 0)

constexpr int SEQ = 2048, DM = 640, NH = 32, DH = 20;
constexpr int TILE_B = 3328;
constexpr int CHUNK_B = 8 * TILE_B;
constexpr int QT_B = 1280;
constexpr size_t WS_KV = 0;
constexpr size_t WS_Q = WS_KV + (size_t)2 * 32 * 64 * TILE_B;
constexpr size_t WS_O = WS_Q + (size_t)2 * 32 * 64 * QT_B;
constexpr size_t WS_G = WS_O + (size_t)2 * SEQ * DM * 2;

typedef __attribute__((ext_vector_type(2))) __bf16 bf16x2_t;
typedef __attribute__((ext_vector_type(2))) float f32x2_t;
DI unsigned cvtpk(float lo, float hi) { const f32x2_t v = {lo, hi}; return __builtin_bit_cast(unsigned, __builtin_convertvector(v, bf16x2_t)); }
DI float bf2f(u16 v) { return __uint_as_float(((unsigned)v) << 16); }
DI float rbf(float x) { return __uint_as_float(cvtpk(x, x) << 16); }

DI void glds16(const void* g, unsigned lds_base) {
    unsigned sv; asm volatile("s_mov_b32 %0, m0\n\ts_mov_b32 m0, %2\n\ts_nop 0\n\tglobal_load_lds_dwordx4 %1, off\n\ts_mov_b32 m0, %0" : "=&s"(sv) : "v"(g), "s"(lds_base) : "memory"); }
DI void glds4(const void* g, unsigned lds_base) {
    unsigned sv; asm volatile("s_mov_b32 %0, m0\n\ts_mov_b32 m0, %2\n\ts_nop 0\n\tglobal_load_lds_dword %1, off\n\ts_mov_b32 m0, %0" : "=&s"(sv) : "v"(g), "s"(lds_base) : "memory"); }
DI void glds16s(unsigned voff, const void* sbase, unsigned lds_base) {
    unsigned sv; asm volatile("s_mov_b32 %0, m0\n\ts_mov_b32 m0, %3\n\ts_nop 0\n\tglobal_load_lds_dwordx4 %1, %2\n\ts_mov_b32 m0, %0" : "=&s"(sv) : "v"(voff), "s"(sbase), "s"(lds_base) : "memory"); }
DI void glds4s(unsigned voff, const void* sbase, unsigned lds_base) {
    unsigned sv; asm volatile("s_mov_b32 %0, m0\n\ts_mov_b32 m0, %3\n\ts_nop 0\n\tglobal_load_lds_dword %1, %2\n\ts_mov_b32 m0, %0" : "=&s"(sv) : "v"(voff), "s"(sbase), "s"(lds_base) : "memory"); }
#define L2TOUCH(p, dummy_lds) glds4((p), (dummy_lds))
DI bf16x4 trread(lds_cptr p) { return __builtin_bit_cast(bf16x4, __builtin_amdgcn_ds_read_tr16_b64_v4i16((__attribute__((address_space(3))) v4i16_t*)p)); }
DI bf16x8 ldsr128(lds_cptr p) { return *(const __attribute__((address_space(3))) bf16x8*)p; }
DI bf16x4 ldsr64(lds_cptr p) { return *(const __attribute__((address_space(3))) bf16x4*)p; }
#define WAIT_BAR0() asm volatile("s_waitcnt vmcnt(0) lgkmcnt(0)\n\ts_barrier" ::: "memory")

constexpr int K1_XB = 256 * 128;
constexpr int K1_WB = 64 * 256;
constexpr int K1_NB = 3;

DI void k1_loader(const u16* __restrict__ X, const u16* __restrict__ W, char* lds, int m0, int n0, int lw) {
    const int lane = threadIdx.x & 63;
    const unsigned lds0 = (unsigned)(uintptr_t)lds;
    unsigned xvo[2];
#pragma unroll
    for (int par = 0; par < 2; ++par) { const int r = lane >> 3; const int c = (lane & 7) ^ ((4 * par + (r >> 1)) & 7); xvo[par] = r * 1280 + c * 16; }
    const unsigned wvo = (lane >> 4) * 3840 + (((lane & 15) ^ (((lane >> 4) & 3) << 2)) * 16);
    const char* xbase = (const char*)X + (size_t)(m0 + 64 * lw) * 1280;
    const char* wbase = (const char*)W + (size_t)(16 * lw) * 3840 + n0 * 2;
    auto stage = [&](int kt, int buf) {
        const unsigned xb = lds0 + buf * K1_XB + lw * 8192, wb = lds0 + K1_NB * K1_XB + buf * K1_WB + lw * 4096;
        const char* xs = xbase + kt * 128; const char* wsr = wbase + (size_t)kt * 64 * 3840;
#pragma unroll
        for (int j = 0; j < 4; ++j) glds16s(wvo, wsr + (size_t)j * 4 * 3840, wb + j * 1024);
#pragma unroll
        for (int i = 0; i < 8; ++i) glds16s(xvo[i & 1], xs + (size_t)i * 8 * 1280, xb + i * 1024);
    };
    stage(0, 0); stage(1, 1);
    int pb = 2;
#pragma unroll 1
    for (int kt = 0; kt < 10; ++kt) {
        if (kt < 9) asm volatile("s_waitcnt vmcnt(12)\n\ts_barrier" ::: "memory"); else asm volatile("s_waitcnt vmcnt(0)\n\ts_barrier" ::: "memory");
        if (kt + 2 < 10) stage(kt + 2, pb);
        pb = (pb == 2) ? 0 : pb + 1;
    }
}

template <bool VT>
DI void k1_body(char* __restrict__ ws, char* lds, int m0, int n0, int branch, int type) {
    const int tid = threadIdx.x, lane = tid & 63, hh = lane >> 5, l31 = lane & 31;
    const int wid = __builtin_amdgcn_readfirstlane(tid >> 6);
    const int wm = wid >> 1, wn = wid & 1;
    const int sw = (l31 >> 1) & 7;
    int xoff[4];
#pragma unroll
    for (int ks = 0; ks < 4; ++ks) xoff[ks] = (wm * 64 + l31) * 128 + (((2 * ks + hh) ^ sw) * 16);
    const int q = (lane & 15) >> 2, p = lane & 3, ch = (lane >> 4) & 1;
    int woff[2];
#pragma unroll
    for (int ni = 0; ni < 2; ++ni) woff[ni] = K1_NB * K1_XB + (8 * hh + q) * 256 + (((wn * 2 + ni) ^ q) * 64) + ch * 32 + p * 8;

    f32x16 acc[2][2];
#pragma unroll
    for (int a = 0; a < 2; ++a)
#pragma unroll
        for (int b = 0; b < 2; ++b) acc[a][b] = f32x16{};
    struct Frags { bf16x8 x0, x1, w0, w1; };
    auto ldfr = [&](int buf, int ks) {
        const lds_cptr xb = (lds_cptr)lds + buf * K1_XB + xoff[ks], wb0 = (lds_cptr)lds + buf * K1_WB + woff[0] + ks * 4096, wb1 = (lds_cptr)lds + buf * K1_WB + woff[1] + ks * 4096;
        Frags f;
        f.x0 = ldsr128(xb); f.w0 = __builtin_shufflevector(trread(wb0), trread(wb0 + 1024), 0, 1, 2, 3, 4, 5, 6, 7);
        f.w1 = __builtin_shufflevector(trread(wb1), trread(wb1 + 1024), 0, 1, 2, 3, 4, 5, 6, 7); f.x1 = ldsr128(xb + 4096);
        return f;
    };
    auto mma = [&](const Frags& f) {
        if (VT) { acc[0][0] = MFMA32(f.x0, f.w0, acc[0][0]); acc[0][1] = MFMA32(f.x0, f.w1, acc[0][1]); acc[1][0] = MFMA32(f.x1, f.w0, acc[1][0]); acc[1][1] = MFMA32(f.x1, f.w1, acc[1][1]); }
        else    { acc[0][0] = MFMA32(f.w0, f.x0, acc[0][0]); acc[0][1] = MFMA32(f.w1, f.x0, acc[0][1]); acc[1][0] = MFMA32(f.w0, f.x1, acc[1][0]); acc[1][1] = MFMA32(f.w1, f.x1, acc[1][1]); }
    };
#define K1SB() __builtin_amdgcn_sched_barrier(0)
    int buf = 0;
    Frags fa, fb, fc;
#pragma unroll 1
    for (int kt = 0; kt < 10; ++kt) {
        asm volatile("s_waitcnt lgkmcnt(0)\n\ts_barrier" ::: "memory");
        fa = ldfr(buf, 0); fb = ldfr(buf, 1); K1SB();
        if (kt > 0) mma(fc); K1SB();
        mma(fa); K1SB();
        fa = ldfr(buf, 2); K1SB(); mma(fb); K1SB();
        fc = ldfr(buf, 3); K1SB(); mma(fa); K1SB();
        buf = (buf == 2) ? 0 : buf + 1;
    }
    mma(fc);
#undef K1SB
    const int fb0 = (n0 - type * 640) + wn * 64;
    if (VT) {
        char* kv = ws + WS_KV + (size_t)branch * 32 * 64 * TILE_B;
#pragma unroll
        for (int ni = 0; ni < 2; ++ni) {
            const int f = fb0 + ni * 32 + l31, head = f / 20, d = f - head * 20;
#pragma unroll
            for (int mi = 0; mi < 2; ++mi) {
                const int kt = (m0 >> 5) + wm * 2 + mi;
                char* dst = kv + ((size_t)head * 64 + kt) * TILE_B + 1280 + hh * 512 + d * 16;
#pragma unroll
                for (int s = 0; s < 2; ++s) {
                    const f32x16& a = acc[mi][ni];
                    u32x4 v = {cvtpk(a[8 * s], a[8 * s + 1]), cvtpk(a[8 * s + 2], a[8 * s + 3]), cvtpk(a[8 * s + 4], a[8 * s + 5]), cvtpk(a[8 * s + 6], a[8 * s + 7])};
                    *(u32x4*)(dst + s * 1024) = v;
                    if (d == 19) *(u32x4*)(dst + s * 1024 + 16) = (u32x4){0x3f803f80u, 0x3f803f80u, 0x3f803f80u, 0x3f803f80u};
                }
            }
        }
    } else {
        const float sc = (type == 0) ? 0.32259857558f : 1.0f;
        char* base = (type == 0) ? (ws + WS_Q + (size_t)branch * 32 * 64 * QT_B) : (ws + WS_KV + (size_t)branch * 32 * 64 * TILE_B);
        const int rec = (type == 0) ? QT_B : TILE_B;
#pragma unroll
        for (int ni = 0; ni < 2; ++ni)
#pragma unroll
            for (int g = 0; g < 4; ++g) {
                const int f0 = fb0 + ni * 32 + 8 * g + 4 * hh, head = f0 / 20, d0 = f0 - head * 20;
                const int off = (d0 < 16) ? ((d0 >> 3) * 512 + l31 * 16 + (d0 & 7) * 2) : (1024 + l31 * 8);
#pragma unroll
                for (int mi = 0; mi < 2; ++mi) {
                    const int tt = (m0 >> 5) + wm * 2 + mi;
                    const f32x16& a = acc[mi][ni];
                    u32x2 v = {cvtpk(a[4 * g] * sc, a[4 * g + 1] * sc), cvtpk(a[4 * g + 2] * sc, a[4 * g + 3] * sc)};
                    *(u32x2*)(base + ((size_t)head * 64 + tt) * rec + off) = v;
                }
            }
    }
}

__global__ __launch_bounds__(768, 3) void k1_qkv(const u16* __restrict__ X, const u16* __restrict__ Wa, const u16* __restrict__ Wb, const u16* __restrict__ gw, char* __restrict__ ws) {
    __shared__ __attribute__((aligned(16))) char lds[K1_NB * (K1_XB + K1_WB)];
    const int bid = blockIdx.x;
    if (bid >= 240) {
        if (threadIdx.x >= 512) return;
        const int tok = (bid - 240) * 128 + (threadIdx.x >> 2), part = threadIdx.x & 3;
        const u16* xr = X + (size_t)tok * DM + part * 160; const u16* gr = gw + part * 160;
        float s = 0.f;
#pragma unroll 4
        for (int i = 0; i < 20; ++i) {
            const bf16x8 xv = *(const bf16x8*)(xr + i * 8), gv = *(const bf16x8*)(gr + i * 8);
#pragma unroll
            for (int j = 0; j < 8; ++j) s += rbf(bf2f((u16)xv[j]) * bf2f((u16)gv[j]));
        }
        s += __shfl_xor(s, 1); s += __shfl_xor(s, 2);
        if (part == 0) { const float z = rbf(s); ((float*)(ws + WS_G))[tok] = rbf(1.0f / (1.0f + __expf(-z))); }
        return;
    }
    const int xcd = bid & 7, idx = bid >> 3;
    const int branch = xcd >> 2, mt = (xcd & 3) * 2 + (idx / 15), nt = idx % 15, type = nt / 5;
    const u16* W = branch ? Wb : Wa;
    const int wv = __builtin_amdgcn_readfirstlane(threadIdx.x >> 6);
    if (wv >= 8) { k1_loader(X, W, lds, mt * 256, nt * 128, wv - 8); return; }
    if (type == 2) k1_body<true>(ws, lds, mt * 256, nt * 128, branch, type);
    else k1_body<false>(ws, lds, mt * 256, nt * 128, branch, type);
}

constexpr int BIG_B = 16 * TILE_B;
constexpr int K2_LDS = 3 * BIG_B + 64;
constexpr float REF_OFF = 90.0f;
#define SB() __builtin_amdgcn_sched_barrier(0)
#define PIN(x) asm volatile("" : "+v"(x))
#define EX(v) __builtin_amdgcn_exp2f(v)

DI float fmax16(const f32x16& s) {
    float a = fmaxf(fmaxf(s[0], s[1]), s[2]), b = fmaxf(fmaxf(s[3], s[4]), s[5]);
    a = fmaxf(fmaxf(a, s[6]), s[7]); b = fmaxf(fmaxf(b, s[8]), s[9]);
    a = fmaxf(fmaxf(a, s[10]), s[11]); b = fmaxf(fmaxf(b, s[12]), s[13]);
    a = fmaxf(fmaxf(a, s[14]), s[15]);
    return fmaxf(a, b);
}
DI f32x16 splat16(float v) { f32x16 r;
#pragma unroll
    for (int i = 0; i < 16; ++i) r[i] = v; return r; }
template <bool NEXT, bool PREV>
DI void phase(f32x16& X, f32x16& o, const bf16x8& vf0, f32x16& Y, const bf16x8& kf0, const bf16x4& kf1, const bf16x8& qy0, const bf16x4& qy1, const f32x16& nmy,
              f32x16& op, const bf16x8& vp, u32x4& pp) {
    SB();
    X[0] = EX(X[0]); X[1] = EX(X[1]); PIN(X); SB();
    if (PREV) { op = MFMA32(vp, __builtin_bit_cast(bf16x8, pp), op); PIN(op); SB(); }
    X[2] = EX(X[2]); X[3] = EX(X[3]); X[4] = EX(X[4]); X[5] = EX(X[5]); X[6] = EX(X[6]); PIN(X); SB();
    if (NEXT) { Y = MFMA32(kf0, qy0, nmy); PIN(Y); SB(); }
    X[7] = EX(X[7]); PIN(X); SB();
    u32x4 p0 = {cvtpk(X[0], X[1]), cvtpk(X[2], X[3]), cvtpk(X[4], X[5]), cvtpk(X[6], X[7])}; PIN(p0); SB();
    X[8] = EX(X[8]); PIN(X); SB();
    if (NEXT) { Y = MFMA32K8(kf1, qy1, Y); PIN(Y); SB(); }
    X[9] = EX(X[9]); X[10] = EX(X[10]); X[11] = EX(X[11]); X[12] = EX(X[12]); PIN(X); SB();
    o = MFMA32(vf0, __builtin_bit_cast(bf16x8, p0), o); PIN(o); SB();
    X[13] = EX(X[13]); X[14] = EX(X[14]); X[15] = EX(X[15]); PIN(X); SB();
    pp = (u32x4){cvtpk(X[8], X[9]), cvtpk(X[10], X[11]), cvtpk(X[12], X[13]), cvtpk(X[14], X[15])}; PIN(pp); SB();
}
DI bool bad_f(float x) { return !(fabsf(x) <= 1.0e37f); }
DI void store_o(const f32x16& o, char* ws, int branch, int head, int qtile, int lane) {
    const int hh = lane >> 5, l31 = lane & 31;
    float l = o[8];
    l = __shfl(l, 32 + l31);
    const float rl = __builtin_amdgcn_rcpf(l);
    const int tok = qtile * 32 + l31;
    char* base = ws + WS_O + ((size_t)(branch * 128 + (tok >> 4)) * 20) * 1024 + (tok & 15) * 16;
#pragma unroll
    for (int g = 0; g < 3; ++g) {
        if (g == 2 && hh) break;
        const int k0 = head * 20 + 8 * g + 4 * hh;
        const int ks = k0 >> 5, kk = k0 & 31;
        u32x2 v = {cvtpk(o[4 * g] * rl, o[4 * g + 1] * rl), cvtpk(o[4 * g + 2] * rl, o[4 * g + 3] * rl)};
        *(u32x2*)(base + ks * 1024 + ((kk & 15) >> 2) * 256 + (kk >> 4) * 8) = v;
    }
}

__global__ __launch_bounds__(512, 2) void k2_attn(char* __restrict__ ws, const char* __restrict__ Woa, const char* __restrict__ Wob) {
    __shared__ __attribute__((aligned(16))) char lds[K2_LDS];
    const int tid = threadIdx.x, lane = tid & 63, hh = lane >> 5, l31 = lane & 31;
    const int wid = __builtin_amdgcn_readfirstlane(tid >> 6);
    const int bid = blockIdx.x;
    const int head = (bid & 7) * 4 + ((bid >> 3) & 3), qb = bid >> 5;
    const unsigned lds0 = (unsigned)(uintptr_t)lds;
    const char* kvA = ws + WS_KV + (size_t)head * 64 * TILE_B;
    const char* kvW = ws + WS_KV + ((size_t)(32 + head) * 64 + qb * 8 - 8) * TILE_B;
    volatile int* flag = (volatile int*)(lds + 3 * BIG_B);
    const int pr = wid >> 1, kh = wid & 1;
    const lds_cptr l16 = (lds_cptr)lds + lane * 16, l8 = (lds_cptr)lds + 1024 + l31 * 8;
    const unsigned vo16 = wid * 1024 + lane * 16;
#define ROUND(src, slotbase, r) glds16s(vo16, (src) + (r) * 8192, (slotbase) + wid * 1024 + (r) * 8192)
#define ROUND6(src, slotbase) do { if (wid < 4) ROUND(src, slotbase, 6); } while (0)
#define ALLROUNDS(src, slotbase) do { ROUND(src, slotbase, 0); ROUND(src, slotbase, 1); ROUND(src, slotbase, 2); ROUND(src, slotbase, 3); ROUND(src, slotbase, 4); ROUND(src, slotbase, 5); ROUND6(src, slotbase); } while (0)

    ALLROUNDS(kvA, lds0);
    bf16x8 qa0, qb0, qw0; bf16x4 qa1, qb1, qw1;
    {
        const char* qp = ws + WS_Q + ((size_t)head * 64 + qb * 8 + 2 * pr) * QT_B;
        const char* qq = ws + WS_Q + ((size_t)(32 + head) * 64 + qb * 8 + wid) * QT_B;
        qa0 = *(const bf16x8*)(qp + lane * 16); qb0 = *(const bf16x8*)(qp + QT_B + lane * 16); qw0 = *(const bf16x8*)(qq + lane * 16);
        const bf16x4 ta = *(const bf16x4*)(qp + 1024 + l31 * 8), tb = *(const bf16x4*)(qp + QT_B + 1024 + l31 * 8), tw = *(const bf16x4*)(qq + 1024 + l31 * 8);
        qa1 = hh ? bf16x4{0, 0, 0, 0} : ta; qb1 = hh ? bf16x4{0, 0, 0, 0} : tb; qw1 = hh ? bf16x4{0, 0, 0, 0} : tw;
    }
    if (tid == 0) *flag = 0;
    WAIT_BAR0();
    float ma, mb;
    {
        const bf16x8 kf0 = ldsr128(l16); const bf16x4 kf1 = ldsr64(l8);
        f32x16 s = MFMA32(kf0, qa0, f32x16{}); s = MFMA32K8(kf1, qa1, s);
        f32x16 u = MFMA32(kf0, qb0, f32x16{}); u = MFMA32K8(kf1, qb1, u);
        ma = fmax16(s); ma = fmaxf(ma, __shfl_xor(ma, 32)) + REF_OFF;
        mb = fmax16(u); mb = fmaxf(mb, __shfl_xor(mb, 32)) + REF_OFF;
    }
    f32x16 keep;
    for (int attempt = 0;; ++attempt) {
        const f32x16 nma = splat16(-ma), nmb = splat16(-mb);
        f32x16 o0 = f32x16{}, o1 = f32x16{}, S0, S1;
        int cur = kh * 8 * TILE_B;
        bf16x8 kf0 = ldsr128(l16 + cur), vf0 = ldsr128(l16 + cur + 1280), vf1 = ldsr128(l16 + cur + 2304); bf16x4 kf1 = ldsr64(l8 + cur);
        S0 = MFMA32(kf0, qa0, nma); S0 = MFMA32K8(kf1, qa1, S0);
        bf16x8 vprev = vf1; u32x4 pp = {0u, 0u, 0u, 0u};
        int slot = 0;
#pragma unroll 1
        for (int c = 0; c < 4; ++c) {
            const int nslot = (slot == 2) ? 0 : slot + 1;
            const int nxt = nslot * BIG_B + kh * 8 * TILE_B;
            const char* dsrc = (c < 3) ? kvA + (size_t)(c + 1) * BIG_B : kvW;
            const unsigned ddst = lds0 + nslot * BIG_B;
#pragma unroll
            for (int t = 0; t < 8; ++t) {
                if (t == 7) asm volatile("s_waitcnt vmcnt(0)\n\ts_barrier" ::: "memory");
                const int no = (t < 7) ? cur + (t + 1) * TILE_B : nxt;
                const bf16x8 nk0 = ldsr128(l16 + no); const bf16x4 nk1 = ldsr64(l8 + no);
                if (t < 3) { ROUND(dsrc, ddst, 2 * t); ROUND(dsrc, ddst, 2 * t + 1); }
                if (t == 3) ROUND6(dsrc, ddst);
                phase<true, true>(S0, o0, vf0, S1, kf0, kf1, qb0, qb1, nmb, o1, vprev, pp);
                const bf16x8 nv0 = ldsr128(l16 + no + 1280), nv1 = ldsr128(l16 + no + 2304);
                phase<true, true>(S1, o1, vf0, S0, nk0, nk1, qa0, qa1, nma, o0, vf1, pp);
                vprev = vf1; kf0 = nk0; kf1 = nk1; vf0 = nv0; vf1 = nv1;
            }
            cur = nxt; slot = nslot;
        }
        o1 = MFMA32(vprev, __builtin_bit_cast(bf16x8, pp), o1);
        {
            float* ex = (float*)(lds + 2 * BIG_B) + wid * 12 * 64;
            bool bad = false;
#pragma unroll
            for (int r = 0; r < 12; ++r) {
                ex[r * 64 + lane] = kh ? o0[r] : o1[r]; keep[r] = kh ? o1[r] : o0[r];
                if (r < 9 || !hh) bad |= bad_f(o0[r]) | bad_f(o1[r]);
            }
            if (attempt == 0 && bad) *flag = 1;
        }
        if (attempt == 0) {
            const int wb = BIG_B + wid * TILE_B;
            const int i0 = (qb == 0) ? (8 - wid) : 0;
            float m;
            {
                const int off = wb + 8 * TILE_B;
                f32x16 s = MFMA32(ldsr128(l16 + off), qw0, f32x16{}); s = MFMA32K8(ldsr64(l8 + off), qw1, s);
#pragma unroll
                for (int r = 0; r < 16; ++r) { const int kl = (r & 3) + 8 * (r >> 2) + 4 * hh; if (kl > l31) s[r] = -3.0e38f; }
                m = fmax16(s); m = fmaxf(m, __shfl_xor(m, 32));
            }
            float mr = m + REF_OFF;
            f32x16 ob;
            for (int att = 0; att < 2; ++att) {
                if (att == 1) {
                    for (int i = i0; i < 8; ++i) {
                        const int off = wb + i * TILE_B;
                        f32x16 s = MFMA32(ldsr128(l16 + off), qw0, f32x16{}); s = MFMA32K8(ldsr64(l8 + off), qw1, s);
                        if (i == 0) {
#pragma unroll
                            for (int r = 0; r < 16; ++r) { const int kl = (r & 3) + 8 * (r >> 2) + 4 * hh; if (kl <= l31) s[r] = -3.0e38f; }
                        }
                        m = fmaxf(m, fmax16(s));
                    }
                    mr = fmaxf(m, __shfl_xor(m, 32));
                }
                const f32x16 nm = splat16(-mr);
                ob = f32x16{};
                f32x16 sa, sb;
                int off = wb + i0 * TILE_B;
                bf16x8 wv0 = ldsr128(l16 + off + 1280), wv1 = ldsr128(l16 + off + 2304), wvp = wv1;
                u32x4 wp = {0u, 0u, 0u, 0u};
                sa = MFMA32(ldsr128(l16 + off), qw0, nm); sa = MFMA32K8(ldsr64(l8 + off), qw1, sa);
                if (i0 == 0) {
#pragma unroll
                    for (int r = 0; r < 16; ++r) { const int kl = (r & 3) + 8 * (r >> 2) + 4 * hh; if (kl <= l31) sa[r] = -INFINITY; }
                }
                int i = i0;
                bool in_a = true;
                while (i < 8) {
                    {   off += TILE_B;
                        const bf16x8 nk0 = ldsr128(l16 + off), nv0 = ldsr128(l16 + off + 1280), nv1 = ldsr128(l16 + off + 2304); const bf16x4 nk1 = ldsr64(l8 + off);
                        phase<true, true>(sa, ob, wv0, sb, nk0, nk1, qw0, qw1, nm, ob, wvp, wp);
                        wvp = wv1; wv0 = nv0; wv1 = nv1; ++i; }
                    if (i == 8) { in_a = false; break; }
                    {   off += TILE_B;
                        const bf16x8 nk0 = ldsr128(l16 + off), nv0 = ldsr128(l16 + off + 1280), nv1 = ldsr128(l16 + off + 2304); const bf16x4 nk1 = ldsr64(l8 + off);
                        phase<true, true>(sb, ob, wv0, sa, nk0, nk1, qw0, qw1, nm, ob, wvp, wp);
                        wvp = wv1; wv0 = nv0; wv1 = nv1; ++i; }
                }
                if (!in_a) sa = sb;
#pragma unroll
                for (int r = 0; r < 16; ++r) { const int kl = (r & 3) + 8 * (r >> 2) + 4 * hh; if (kl > l31) sa[r] = -INFINITY; }
                phase<false, true>(sa, ob, wv0, sb, wv0, qw1, qw0, qw1, nm, ob, wvp, wp);
                ob = MFMA32(wv1, __builtin_bit_cast(bf16x8, wp), ob);
                bool bad = false;
#pragma unroll
                for (int r = 0; r < 12; ++r) if (r < 9 || !hh) bad |= bad_f(ob[r]);
                if (att == 1 || !__any(bad)) break;
            }
            store_o(ob, ws, 1, head, qb * 8 + wid, lane);
        }
        WAIT_BAR0();
        if (attempt == 1 || *flag == 0) {
            const float* in = (const float*)(lds + 2 * BIG_B) + (wid ^ 1) * 12 * 64;
#pragma unroll
            for (int r = 0; r < 12; ++r) keep[r] += in[r * 64 + lane];
            break;
        }
        ma = -3.0e38f; mb = -3.0e38f;
        ALLROUNDS(kvA, lds0);
        WAIT_BAR0();
        for (int c = 0; c < 4; ++c) {
            if (c < 3) { const char* nsrc = kvA + (size_t)(c + 1) * BIG_B; const unsigned nd = lds0 + ((c + 1) & 1) * BIG_B; ALLROUNDS(nsrc, nd); }
#pragma unroll 1
            for (int t = 0; t < 16; ++t) {
                const int off = (c & 1) * BIG_B + t * TILE_B;
                const bf16x8 kf0 = ldsr128(l16 + off); const bf16x4 kf1 = ldsr64(l8 + off);
                f32x16 s = MFMA32(kf0, qa0, f32x16{}); s = MFMA32K8(kf1, qa1, s);
                f32x16 u = MFMA32(kf0, qb0, f32x16{}); u = MFMA32K8(kf1, qb1, u);
                ma = fmaxf(ma, fmax16(s)); mb = fmaxf(mb, fmax16(u));
            }
            WAIT_BAR0();
        }
        ma = fmaxf(ma, __shfl_xor(ma, 32)); mb = fmaxf(mb, __shfl_xor(mb, 32));
        ALLROUNDS(kvA, lds0);
        WAIT_BAR0();
    }
    store_o(keep, ws, 0, head, qb * 8 + 2 * pr + kh, lane);
#undef ROUND
#undef ROUND6
#undef ALLROUNDS
}

constexpr int K3_CB = 2 * 160 * 160;

__global__ __launch_bounds__(512, 2) void k3_out(const u16* __restrict__ Woa, const u16* __restrict__ Wob, const char* __restrict__ ws, u16* __restrict__ out) {
    __shared__ __attribute__((aligned(16))) char lds[3 * K3_CB + 6144];
    const int tid = threadIdx.x, lane = tid & 63;
    const int wid = __builtin_amdgcn_readfirstlane(tid >> 6);
    const int bid = blockIdx.x;
    const int ft = (bid >> 3) & 7, mt = (bid & 7) * 4 + (bid >> 6);
    const int n0 = ft * 80, tt = wid & 3, br = wid >> 2;
    const unsigned lds0 = (unsigned)(uintptr_t)lds;
    const u16* wsrc[7];
#pragma unroll
    for (int i = 0; i < 7; ++i) {
        int u = (wid + 8 * i) * 64 + lane; if (u >= 3200) u = lane;
        const int b = u / 1600, rem = u - b * 1600, row = rem / 10, c = rem - row * 10;
        wsrc[i] = (b ? Wob : Woa) + (size_t)row * DM + n0 + c * 8;
    }
    const unsigned last_dst = (wid < 2) ? (unsigned)((wid + 48) * 1024) : 0xffffffffu;
    auto stage = [&](int ck, int buf) {
#pragma unroll
        for (int i = 0; i < 6; ++i) glds16(wsrc[i] + (size_t)ck * 160 * DM, lds0 + buf * K3_CB + (wid + 8 * i) * 1024);
        glds16(wsrc[6] + (size_t)ck * 160 * DM, (wid < 2) ? lds0 + buf * K3_CB + last_dst : lds0 + 3 * K3_CB + (wid - 2) * 1024);
    };
    const int tile16 = mt * 4 + tt;
    const char* op = ws + WS_O + ((size_t)(br * 128 + tile16) * 20) * 1024 + lane * 16;
    bf16x8 of[20];
#define LDO5(b) do { const char* q_ = op + (b) * 1024; \
        asm volatile("global_load_dwordx4 %0, %5, off\n\tglobal_load_dwordx4 %1, %5, off offset:1024\n\tglobal_load_dwordx4 %2, %5, off offset:2048\n\tglobal_load_dwordx4 %3, %5, off offset:3072\n\tglobal_load_dwordx4 %4, %6, off" \
                     : "=&v"(of[(b)]), "=&v"(of[(b) + 1]), "=&v"(of[(b) + 2]), "=&v"(of[(b) + 3]), "=&v"(of[(b) + 4]) : "v"(q_), "v"(q_ + 4096) : "memory"); } while (0)
#define WAITO(str, b) asm volatile(str "\n\ts_barrier" : "+v"(of[(b)]), "+v"(of[(b) + 1]), "+v"(of[(b) + 2]), "+v"(of[(b) + 3]), "+v"(of[(b) + 4]) :: "memory")
    stage(0, 0); LDO5(0);
    stage(1, 1); LDO5(5);
    const int tok = tile16 * 16 + (lane & 15);
    const int q = (lane & 15) >> 2, p = lane & 3, kq = lane >> 4;
    const int woff = br * 25600 + (4 * kq + q) * 160 + p * 8;
    f32x4 acc[5];
#pragma unroll
    for (int f = 0; f < 5; ++f) acc[f] = f32x4{};
    auto compute = [&](int ck) {
        const lds_cptr wb = (lds_cptr)lds + (ck % 3) * K3_CB + woff;
#pragma unroll
        for (int ks = 0; ks < 5; ++ks)
#pragma unroll
            for (int f = 0; f < 5; ++f) {
                const bf16x4 lo = trread(wb + ks * 5120 + f * 32), hi = trread(wb + ks * 5120 + 2560 + f * 32);
                acc[f] = MFMA16(__builtin_shufflevector(lo, hi, 0, 1, 2, 3, 4, 5, 6, 7), of[ck * 5 + ks], acc[f]);
            }
    };
    WAITO("s_waitcnt vmcnt(12)", 0);
    stage(2, 2); LDO5(10);
    compute(0);
    WAITO("s_waitcnt vmcnt(12) lgkmcnt(0)", 5);
    stage(3, 0); LDO5(15);
    compute(1);
    WAITO("s_waitcnt vmcnt(12)", 10);
    compute(2);
    WAITO("s_waitcnt vmcnt(0) lgkmcnt(0)", 15);
    const float g = ((const float*)(ws + WS_G))[tok];
    compute(3);
#undef LDO5
#undef WAITO
    float* ex = (float*)(lds + K3_CB) + tt * 20 * 64;
    if (br == 1) {
#pragma unroll
        for (int f = 0; f < 5; ++f)
#pragma unroll
            for (int r = 0; r < 4; ++r) ex[(f * 4 + r) * 64 + lane] = acc[f][r];
    }
    __syncthreads();
    if (br == 0) {
        const float g1 = rbf(1.0f - g);
        u16* orow = out + (size_t)tok * DM + n0 + kq * 4;
#pragma unroll
        for (int f = 0; f < 5; ++f) {
            float v[4];
#pragma unroll
            for (int r = 0; r < 4; ++r) v[r] = rbf(g * rbf(acc[f][r])) + rbf(g1 * rbf(ex[(f * 4 + r) * 64 + lane]));
            *(u32x2*)(orow + f * 16) = (u32x2){cvtpk(v[0], v[1]), cvtpk(v[2], v[3])};
        }
    }
}

extern "C" void kernel_launch(void* const* d_in, const int* in_sizes, int n_in, void* d_out, int out_size, void* d_ws, size_t ws_size, hipStream_t stream) {
    const u16* x = (const u16*)d_in[0];
    const u16* wa = (const u16*)d_in[1];
    const u16* woa = (const u16*)d_in[2];
    const u16* wb = (const u16*)d_in[4];
    const u16* wob = (const u16*)d_in[5];
    const u16* gw = (const u16*)d_in[6];
    char* ws = (char*)d_ws;
    k1_qkv<<<256, 768, 0, stream>>>(x, wa, wb, gw, ws);
    k2_attn<<<256, 512, 0, stream>>>(ws, (const char*)woa, (const char*)wob);
    k3_out<<<256, 512, 0, stream>>>(woa, wob, ws, (u16*)d_out);
}
```

```cpp
#include <hip/hip_runtime.h>
#include <hip/hip_bf16.h>
#include <stdint.h>

typedef __attribute__((ext_vector_type(8))) short bf16x8;
typedef __attribute__((ext_vector_type(4))) short bf16x4;
typedef __attribute__((ext_vector_type(16))) float f32x16;
typedef __attribute__((ext_vector_type(4))) float f32x4;
typedef __attribute__((ext_vector_type(4))) unsigned u32x4;
typedef __attribute__((ext_vector_type(2))) unsigned u32x2;
typedef short v4i16_t __attribute__((ext_vector_type(4)));
typedef __attribute__((address_space(3))) const char* lds_cptr;
typedef unsigned short u16;

#define DI __device__ __forceinline__
#define MFMA32(a, b, c) __builtin_amdgcn_mfma_f32_32x32x16_bf16((a), (b), (c), 0, 0, 0)
#define MFMA32K8(a, b, c) __builtin_amdgcn_mfma_f32_32x32x8bf16_1k((a), (b), (c), 0, 0, 0)
#define MFMA16(a, b, c) __builtin_amdgcn_mfma_f32_16x16x32_bf16((a), (b), (c), 0, 0, 0)

constexpr int SEQ = 2048, DM = 640, NH = 32, DH = 20;
constexpr int TILE_B = 3328;
constexpr int CHUNK_B = 8 * TILE_B;
constexpr int QT_B = 1280;
constexpr size_t WS_KV = 0;
constexpr size_t WS_Q = WS_KV + (size_t)2 * 32 * 64 * TILE_B;
constexpr size_t WS_O = WS_Q + (size_t)2 * 32 * 64 * QT_B;
constexpr size_t WS_G = WS_O + (size_t)2 * SEQ * DM * 2;
constexpr size_t WS_WP = WS_G + 8192;

typedef __attribute__((ext_vector_type(2))) __bf16 bf16x2_t;
typedef __attribute__((ext_vector_type(2))) float f32x2_t;
DI unsigned cvtpk(float lo, float hi) { const f32x2_t v = {lo, hi}; return __builtin_bit_cast(unsigned, __builtin_convertvector(v, bf16x2_t)); }
DI float bf2f(u16 v) { return __uint_as_float(((unsigned)v) << 16); }
DI float rbf(float x) { return __uint_as_float(cvtpk(x, x) << 16); }

DI void glds16(const void* g, unsigned lds_base) {
    unsigned sv; asm volatile("s_mov_b32 %0, m0\n\ts_mov_b32 m0, %2\n\ts_nop 0\n\tglobal_load_lds_dwordx4 %1, off\n\ts_mov_b32 m0, %0" : "=&s"(sv) : "v"(g), "s"(lds_base) : "memory"); }
DI void glds4(const void* g, unsigned lds_base) {
    unsigned sv; asm volatile("s_mov_b32 %0, m0\n\ts_mov_b32 m0, %2\n\ts_nop 0\n\tglobal_load_lds_dword %1, off\n\ts_mov_b32 m0, %0" : "=&s"(sv) : "v"(g), "s"(lds_base) : "memory"); }
DI void glds16s(unsigned voff, const void* sbase, unsigned lds_base) {
    unsigned sv; asm volatile("s_mov_b32 %0, m0\n\ts_mov_b32 m0, %3\n\ts_nop 0\n\tglobal_load_lds_dwordx4 %1, %2\n\ts_mov_b32 m0, %0" : "=&s"(sv) : "v"(voff), "s"(sbase), "s"(lds_base) : "memory"); }
DI void glds4s(unsigned voff, const void* sbase, unsigned lds_base) {
    unsigned sv; asm volatile("s_mov_b32 %0, m0\n\ts_mov_b32 m0, %3\n\ts_nop 0\n\tglobal_load_lds_dword %1, %2\n\ts_mov_b32 m0, %0" : "=&s"(sv) : "v"(voff), "s"(sbase), "s"(lds_base) : "memory"); }
#define L2TOUCH(p, dummy_lds) glds4((p), (dummy_lds))
DI bf16x4 trread(lds_cptr p) { return __builtin_bit_cast(bf16x4, __builtin_amdgcn_ds_read_tr16_b64_v4i16((__attribute__((address_space(3))) v4i16_t*)p)); }
DI bf16x8 ldsr128(lds_cptr p) { return *(const __attribute__((address_space(3))) bf16x8*)p; }
DI bf16x4 ldsr64(lds_cptr p) { return *(const __attribute__((address_space(3))) bf16x4*)p; }
#define WAIT_BAR0() asm volatile("s_waitcnt vmcnt(0) lgkmcnt(0)\n\ts_barrier" ::: "memory")

constexpr int K1_XB = 256 * 128;
constexpr int K1_WB = 64 * 256;
constexpr int K1_NB = 3;

DI void k1_loader(const u16* __restrict__ X, const u16* __restrict__ W, const u16* __restrict__ Woa, const u16* __restrict__ Wob, char* __restrict__ ws, char* lds, int m0, int n0, int lw) {
    const int lane = threadIdx.x & 63;
    const unsigned lds0 = (unsigned)(uintptr_t)lds;
    unsigned xvo[2];
#pragma unroll
    for (int par = 0; par < 2; ++par) { const int r = lane >> 3; const int c = (lane & 7) ^ ((4 * par + (r >> 1)) & 7); xvo[par] = r * 1280 + c * 16; }
    const unsigned wvo = (lane >> 4) * 3840 + (((lane & 15) ^ (((lane >> 4) & 3) << 2)) * 16);
    const char* xbase = (const char*)X + (size_t)(m0 + 64 * lw) * 1280;
    const char* wbase = (const char*)W + (size_t)(16 * lw) * 3840 + n0 * 2;
    auto stage = [&](int kt, int buf) {
        const unsigned xb = lds0 + buf * K1_XB + lw * 8192, wb = lds0 + K1_NB * K1_XB + buf * K1_WB + lw * 4096;
        const char* xs = xbase + kt * 128; const char* wsr = wbase + (size_t)kt * 64 * 3840;
#pragma unroll
        for (int j = 0; j < 4; ++j) glds16s(wvo, wsr + (size_t)j * 4 * 3840, wb + j * 1024);
#pragma unroll
        for (int i = 0; i < 8; ++i) glds16s(xvo[i & 1], xs + (size_t)i * 8 * 1280, xb + i * 1024);
    };
    stage(0, 0); stage(1, 1);
    int pb = 2;
#pragma unroll 1
    for (int kt = 0; kt < 8; ++kt) {
        asm volatile("s_waitcnt vmcnt(12)\n\ts_barrier" ::: "memory");
        stage(kt + 2, pb);
        pb = (pb == 2) ? 0 : pb + 1;
    }
    unsigned gv[2][8];
#pragma unroll
    for (int i = 0; i < 2; ++i) {
        const int t = min((int)(blockIdx.x * 4 + lw) * 64 + lane + i * 61440, 102399);
        const int ln = t & 63, fr = t >> 6, f = fr % 5, ks5 = (fr / 5) % 5, b = (fr / 25) & 1, ck = (fr / 50) & 3, ftile = fr / 200;
        const u16* src = (b ? Wob : Woa) + (size_t)(32 * (5 * ck + ks5) + 4 * (ln >> 4)) * DM + ftile * 80 + f * 16 + (ln & 15);
        asm volatile("global_load_ushort %0, %8, off\n\tglobal_load_ushort %1, %8, off offset:1280\n\tglobal_load_ushort %2, %8, off offset:2560\n\tglobal_load_ushort %3, %8, off offset:3840\n\t"
                     "global_load_ushort %4, %9, off\n\tglobal_load_ushort %5, %9, off offset:1280\n\tglobal_load_ushort %6, %9, off offset:2560\n\tglobal_load_ushort %7, %9, off offset:3840"
                     : "=&v"(gv[i][0]), "=&v"(gv[i][1]), "=&v"(gv[i][2]), "=&v"(gv[i][3]), "=&v"(gv[i][4]), "=&v"(gv[i][5]), "=&v"(gv[i][6]), "=&v"(gv[i][7])
                     : "v"(src), "v"(src + 16 * DM) : "memory");
    }
    asm volatile("s_waitcnt vmcnt(28)\n\ts_barrier" ::: "memory");
    asm volatile("s_waitcnt vmcnt(16)\n\ts_barrier" ::: "memory");
    asm volatile("s_waitcnt vmcnt(0)" : "+v"(gv[0][0]), "+v"(gv[0][1]), "+v"(gv[0][2]), "+v"(gv[0][3]), "+v"(gv[0][4]), "+v"(gv[0][5]), "+v"(gv[0][6]), "+v"(gv[0][7]),
                                         "+v"(gv[1][0]), "+v"(gv[1][1]), "+v"(gv[1][2]), "+v"(gv[1][3]), "+v"(gv[1][4]), "+v"(gv[1][5]), "+v"(gv[1][6]), "+v"(gv[1][7]) :: "memory");
#pragma unroll
    for (int i = 0; i < 2; ++i) {
        const int t = (blockIdx.x * 4 + lw) * 64 + lane + i * 61440;
        const u32x4 v = {gv[i][0] | (gv[i][1] << 16), gv[i][2] | (gv[i][3] << 16), gv[i][4] | (gv[i][5] << 16), gv[i][6] | (gv[i][7] << 16)};
        if (t < 102400) *(u32x4*)(ws + WS_WP + (size_t)t * 16) = v;
    }
}

template <bool VT>
DI void k1_body(char* __restrict__ ws, char* lds, int m0, int n0, int branch, int type) {
    const int tid = threadIdx.x, lane = tid & 63, hh = lane >> 5, l31 = lane & 31;
    const int wid = __builtin_amdgcn_readfirstlane(tid >> 6);
    const int wm = wid >> 1, wn = wid & 1;
    const int sw = (l31 >> 1) & 7;
    int xoff[4];
#pragma unroll
    for (int ks = 0; ks < 4; ++ks) xoff[ks] = (wm * 64 + l31) * 128 + (((2 * ks + hh) ^ sw) * 16);
    const int q = (lane & 15) >> 2, p = lane & 3, ch = (lane >> 4) & 1;
    int woff[2];
#pragma unroll
    for (int ni = 0; ni < 2; ++ni) woff[ni] = K1_NB * K1_XB + (8 * hh + q) * 256 + (((wn * 2 + ni) ^ q) * 64) + ch * 32 + p * 8;

    f32x16 acc[2][2];
#pragma unroll
    for (int a = 0; a < 2; ++a)
#pragma unroll
        for (int b = 0; b < 2; ++b) acc[a][b] = f32x16{};
    struct Frags { bf16x8 x0, x1, w0, w1; };
    auto ldfr = [&](int buf, int ks) {
        const lds_cptr xb = (lds_cptr)lds + buf * K1_XB + xoff[ks], wb0 = (lds_cptr)lds + buf * K1_WB + woff[0] + ks * 4096, wb1 = (lds_cptr)lds + buf * K1_WB + woff[1] + ks * 4096;
        Frags f;
        f.x0 = ldsr128(xb); f.w0 = __builtin_shufflevector(trread(wb0), trread(wb0 + 1024), 0, 1, 2, 3, 4, 5, 6, 7);
        f.w1 = __builtin_shufflevector(trread(wb1), trread(wb1 + 1024), 0, 1, 2, 3, 4, 5, 6, 7); f.x1 = ldsr128(xb + 4096);
        return f;
    };
    auto mma = [&](const Frags& f) {
        if (VT) { acc[0][0] = MFMA32(f.x0, f.w0, acc[0][0]); acc[0][1] = MFMA32(f.x0, f.w1, acc[0][1]); acc[1][0] = MFMA32(f.x1, f.w0, acc[1][0]); acc[1][1] = MFMA32(f.x1, f.w1, acc[1][1]); }
        else    { acc[0][0] = MFMA32(f.w0, f.x0, acc[0][0]); acc[0][1] = MFMA32(f.w1, f.x0, acc[0][1]); acc[1][0] = MFMA32(f.w0, f.x1, acc[1][0]); acc[1][1] = MFMA32(f.w1, f.x1, acc[1][1]); }
    };
#define K1SB() __builtin_amdgcn_sched_barrier(0)
    int buf = 0;
    Frags fa, fb, fc;
#pragma unroll 1
    for (int kt = 0; kt < 10; ++kt) {
        asm volatile("s_waitcnt lgkmcnt(0)\n\ts_barrier" ::: "memory");
        fa = ldfr(buf, 0); fb = ldfr(buf, 1); K1SB();
        if (kt > 0) mma(fc); K1SB();
        mma(fa); K1SB();
        fa = ldfr(buf, 2); K1SB(); mma(fb); K1SB();
        fc = ldfr(buf, 3); K1SB(); mma(fa); K1SB();
        buf = (buf == 2) ? 0 : buf + 1;
    }
    mma(fc);
#undef K1SB
    const int fb0 = (n0 - type * 640) + wn * 64;
    if (VT) {
        char* kv = ws + WS_KV + (size_t)branch * 32 * 64 * TILE_B;
#pragma unroll
        for (int ni = 0; ni < 2; ++ni) {
            const int f = fb0 + ni * 32 + l31, head = f / 20, d = f - head * 20;
#pragma unroll
            for (int mi = 0; mi < 2; ++mi) {
                const int kt = (m0 >> 5) + wm * 2 + mi;
                char* dst = kv + ((size_t)head * 64 + kt) * TILE_B + 1280 + hh * 512 + d * 16;
#pragma unroll
                for (int s = 0; s < 2; ++s) {
                    const f32x16& a = acc[mi][ni];
                    u32x4 v = {cvtpk(a[8 * s], a[8 * s + 1]), cvtpk(a[8 * s + 2], a[8 * s + 3]), cvtpk(a[8 * s + 4], a[8 * s + 5]), cvtpk(a[8 * s + 6], a[8 * s + 7])};
                    *(u32x4*)(dst + s * 1024) = v;
                    if (d == 19) *(u32x4*)(dst + s * 1024 + 16) = (u32x4){0x3f803f80u, 0x3f803f80u, 0x3f803f80u, 0x3f803f80u};
                }
            }
        }
    } else {
        const float sc = (type == 0) ? 0.32259857558f : 1.0f;
        char* base = (type == 0) ? (ws + WS_Q + (size_t)branch * 32 * 64 * QT_B) : (ws + WS_KV + (size_t)branch * 32 * 64 * TILE_B);
        const int rec = (type == 0) ? QT_B : TILE_B;
#pragma unroll
        for (int ni = 0; ni < 2; ++ni)
#pragma unroll
            for (int g = 0; g < 4; ++g) {
                const int f0 = fb0 + ni * 32 + 8 * g + 4 * hh, head = f0 / 20, d0 = f0 - head * 20;
                const int off = (d0 < 16) ? ((d0 >> 3) * 512 + l31 * 16 + (d0 & 7) * 2) : (1024 + l31 * 8);
#pragma unroll
                for (int mi = 0; mi < 2; ++mi) {
                    const int tt = (m0 >> 5) + wm * 2 + mi;
                    const f32x16& a = acc[mi][ni];
                    u32x2 v = {cvtpk(a[4 * g] * sc, a[4 * g + 1] * sc), cvtpk(a[4 * g + 2] * sc, a[4 * g + 3] * sc)};
                    *(u32x2*)(base + ((size_t)head * 64 + tt) * rec + off) = v;
                }
            }
    }
}

__global__ __launch_bounds__(768, 3) void k1_qkv(const u16* __restrict__ X, const u16* __restrict__ Wa, const u16* __restrict__ Wb, const u16* __restrict__ gw,
                                                 const u16* __restrict__ Woa, const u16* __restrict__ Wob, char* __restrict__ ws) {
    __shared__ __attribute__((aligned(16))) char lds[K1_NB * (K1_XB + K1_WB)];
    const int bid = blockIdx.x;
    if (bid >= 240) {
        if (threadIdx.x >= 512) return;
        const int tok = (bid - 240) * 128 + (threadIdx.x >> 2), part = threadIdx.x & 3;
        const u16* xr = X + (size_t)tok * DM + part * 160; const u16* gr = gw + part * 160;
        float s = 0.f;
#pragma unroll 4
        for (int i = 0; i < 20; ++i) {
            const bf16x8 xv = *(const bf16x8*)(xr + i * 8), gv = *(const bf16x8*)(gr + i * 8);
#pragma unroll
            for (int j = 0; j < 8; ++j) s += rbf(bf2f((u16)xv[j]) * bf2f((u16)gv[j]));
        }
        s += __shfl_xor(s, 1); s += __shfl_xor(s, 2);
        if (part == 0) { const float z = rbf(s); ((float*)(ws + WS_G))[tok] = rbf(1.0f / (1.0f + __expf(-z))); }
        return;
    }
    const int xcd = bid & 7, idx = bid >> 3;
    const int branch = xcd >> 2, mt = (xcd & 3) * 2 + (idx / 15), nt = idx % 15, type = nt / 5;
    const u16* W = branch ? Wb : Wa;
    const int wv = __builtin_amdgcn_readfirstlane(threadIdx.x >> 6);
    if (wv >= 8) { k1_loader(X, W, Woa, Wob, ws, lds, mt * 256, nt * 128, wv - 8); return; }
    if (type == 2) k1_body<true>(ws, lds, mt * 256, nt * 128, branch, type);
    else k1_body<false>(ws, lds, mt * 256, nt * 128, branch, type);
}

constexpr int BIG_B = 16 * TILE_B;
constexpr int K2_LDS = 3 * BIG_B + 128 + 1024;
constexpr float REF_OFF = 90.0f;
#define SB() __builtin_amdgcn_sched_barrier(0)
#define PIN(x) asm volatile("" : "+v"(x))
#define EX(v) __builtin_amdgcn_exp2f(v)

DI float fmax16(const f32x16& s) {
    float a = fmaxf(fmaxf(s[0], s[1]), s[2]), b = fmaxf(fmaxf(s[3], s[4]), s[5]);
    a = fmaxf(fmaxf(a, s[6]), s[7]); b = fmaxf(fmaxf(b, s[8]), s[9]);
    a = fmaxf(fmaxf(a, s[10]), s[11]); b = fmaxf(fmaxf(b, s[12]), s[13]);
    a = fmaxf(fmaxf(a, s[14]), s[15]);
    return fmaxf(a, b);
}
DI f32x16 splat16(float v) { f32x16 r;
#pragma unroll
    for (int i = 0; i < 16; ++i) r[i] = v; return r; }
template <bool NEXT, bool PREV>
DI void phase(f32x16& X, f32x16& o, const bf16x8& vf0, f32x16& Y, const bf16x8& kf0, const bf16x4& kf1, const bf16x8& qy0, const bf16x4& qy1, const f32x16& nmy,
              f32x16& op, const bf16x8& vp, u32x4& pp) {
    SB();
    X[0] = EX(X[0]); X[1] = EX(X[1]); PIN(X); SB();
    if (PREV) { op = MFMA32(vp, __builtin_bit_cast(bf16x8, pp), op); PIN(op); SB(); }
    X[2] = EX(X[2]); X[3] = EX(X[3]); X[4] = EX(X[4]); X[5] = EX(X[5]); X[6] = EX(X[6]); PIN(X); SB();
    if (NEXT) { Y = MFMA32(kf0, qy0, nmy); PIN(Y); SB(); }
    X[7] = EX(X[7]); PIN(X); SB();
    u32x4 p0 = {cvtpk(X[0], X[1]), cvtpk(X[2], X[3]), cvtpk(X[4], X[5]), cvtpk(X[6], X[7])}; PIN(p0); SB();
    X[8] = EX(X[8]); PIN(X); SB();
    if (NEXT) { Y = MFMA32K8(kf1, qy1, Y); PIN(Y); SB(); }
    X[9] = EX(X[9]); X[10] = EX(X[10]); X[11] = EX(X[11]); X[12] = EX(X[12]); PIN(X); SB();
    o = MFMA32(vf0, __builtin_bit_cast(bf16x8, p0), o); PIN(o); SB();
    X[13] = EX(X[13]); X[14] = EX(X[14]); X[15] = EX(X[15]); PIN(X); SB();
    pp = (u32x4){cvtpk(X[8], X[9]), cvtpk(X[10], X[11]), cvtpk(X[12], X[13]), cvtpk(X[14], X[15])}; PIN(pp); SB();
}
DI bool bad_f(float x) { return !(fabsf(x) <= 1.0e37f); }
DI void store_o(const f32x16& o, char* ws, int branch, int head, int qtile, int lane) {
    const int hh = lane >> 5, l31 = lane & 31;
    float l = o[8];
    l = __shfl(l, 32 + l31);
    const float rl = __builtin_amdgcn_rcpf(l);
    const int tok = qtile * 32 + l31;
    char* base = ws + WS_O + ((size_t)(branch * 128 + (tok >> 4)) * 20) * 1024 + (tok & 15) * 16;
#pragma unroll
    for (int g = 0; g < 3; ++g) {
        if (g == 2 && hh) break;
        const int k0 = head * 20 + 8 * g + 4 * hh;
        const int ks = k0 >> 5, kk = k0 & 31;
        u32x2 v = {cvtpk(o[4 * g] * rl, o[4 * g + 1] * rl), cvtpk(o[4 * g + 2] * rl, o[4 * g + 3] * rl)};
        *(u32x2*)(base + ks * 1024 + ((kk & 15) >> 2) * 256 + (kk >> 4) * 8) = v;
    }
}

__global__ __launch_bounds__(512, 2) void k2_attn(char* __restrict__ ws) {
    __shared__ __attribute__((aligned(16))) char lds[K2_LDS];
    const int tid = threadIdx.x, lane = tid & 63, hh = lane >> 5, l31 = lane & 31;
    const int wid = __builtin_amdgcn_readfirstlane(tid >> 6);
    const int bid = blockIdx.x;
    const int head = (bid & 7) * 4 + ((bid >> 3) & 3), qb = bid >> 5;
    const unsigned lds0 = (unsigned)(uintptr_t)lds;
    const char* kvA = ws + WS_KV + (size_t)head * 64 * TILE_B;
    const char* kvW = ws + WS_KV + ((size_t)(32 + head) * 64 + qb * 8 - 8) * TILE_B;
    volatile __attribute__((address_space(3))) int* flag = (volatile __attribute__((address_space(3))) int*)((__attribute__((address_space(3))) char*)lds + 3 * BIG_B);
    const int pr = wid >> 1, kh = wid & 1;
    const char* wpf = ws + WS_WP + (size_t)(bid >> 3) * 51200;
    const lds_cptr l16 = (lds_cptr)lds + lane * 16, l8 = (lds_cptr)lds + 1024 + l31 * 8;
    const unsigned vo16 = wid * 1024 + lane * 16;
#define ROUND(src, slotbase, r) glds16s(vo16, (src) + (r) * 8192, (slotbase) + wid * 1024 + (r) * 8192)
#define ROUND6(src, slotbase) do { if (wid < 4) ROUND(src, slotbase, 6); } while (0)
#define ALLROUNDS(src, slotbase) do { ROUND(src, slotbase, 0); ROUND(src, slotbase, 1); ROUND(src, slotbase, 2); ROUND(src, slotbase, 3); ROUND(src, slotbase, 4); ROUND(src, slotbase, 5); ROUND6(src, slotbase); } while (0)

    ALLROUNDS(kvA, lds0);
    bf16x8 qa0, qb0, qw0; bf16x4 qa1, qb1, qw1;
    {
        const char* qp = ws + WS_Q + ((size_t)head * 64 + qb * 8 + 2 * pr) * QT_B;
        const char* qq = ws + WS_Q + ((size_t)(32 + head) * 64 + qb * 8 + wid) * QT_B;
        qa0 = *(const bf16x8*)(qp + lane * 16); qb0 = *(const bf16x8*)(qp + QT_B + lane * 16); qw0 = *(const bf16x8*)(qq + lane * 16);
        const bf16x4 ta = *(const bf16x4*)(qp + 1024 + l31 * 8), tb = *(const bf16x4*)(qp + QT_B + 1024 + l31 * 8), tw = *(const bf16x4*)(qq + 1024 + l31 * 8);
        qa1 = hh ? bf16x4{0, 0, 0, 0} : ta; qb1 = hh ? bf16x4{0, 0, 0, 0} : tb; qw1 = hh ? bf16x4{0, 0, 0, 0} : tw;
    }
    if (tid == 0) *flag = 0;
    WAIT_BAR0();
    float ma, mb;
    {
        const bf16x8 kf0 = ldsr128(l16); const bf16x4 kf1 = ldsr64(l8);
        f32x16 s = MFMA32(kf0, qa0, f32x16{}); s = MFMA32K8(kf1, qa1, s);
        f32x16 u = MFMA32(kf0, qb0, f32x16{}); u = MFMA32K8(kf1, qb1, u);
        ma = fmax16(s); ma = fmaxf(ma, __shfl_xor(ma, 32)) + REF_OFF;
        mb = fmax16(u); mb = fmaxf(mb, __shfl_xor(mb, 32)) + REF_OFF;
    }
    f32x16 keep;
    for (int attempt = 0;; ++attempt) {
        const f32x16 nma = splat16(-ma), nmb = splat16(-mb);
        f32x16 o0 = f32x16{}, o1 = f32x16{}, S0, S1;
        int cur = kh * 8 * TILE_B;
        bf16x8 kf0 = ldsr128(l16 + cur), vf0 = ldsr128(l16 + cur + 1280), vf1 = ldsr128(l16 + cur + 2304); bf16x4 kf1 = ldsr64(l8 + cur);
        S0 = MFMA32(kf0, qa0, nma); S0 = MFMA32K8(kf1, qa1, S0);
        bf16x8 vprev = vf1; u32x4 pp = {0u, 0u, 0u, 0u};
        int slot = 0;
#pragma unroll 1
        for (int c = 0; c < 4; ++c) {
            const int nslot = (slot == 2) ? 0 : slot + 1;
            const int nxt = nslot * BIG_B + kh * 8 * TILE_B;
            const char* dsrc = (c < 3) ? kvA + (size_t)(c + 1) * BIG_B : kvW;
            const unsigned ddst = lds0 + nslot * BIG_B;
            const char* wpre = wpf + (c & 1) * 24576 + (c >> 1) * 2048;
#pragma unroll
            for (int t = 0; t < 8; ++t) {
                if (t == 7) asm volatile("s_waitcnt vmcnt(0)\n\ts_barrier" ::: "memory");
                const int no = (t < 7) ? cur + (t + 1) * TILE_B : nxt;
                const bf16x8 nk0 = ldsr128(l16 + no); const bf16x4 nk1 = ldsr64(l8 + no);
                if (t < 3) { ROUND(dsrc, ddst, 2 * t); ROUND(dsrc, ddst, 2 * t + 1); }
                if (t == 3) ROUND6(dsrc, ddst);
                if (t >= 3 && t < 6) glds16s(vo16, wpre + (t - 3) * 8192, lds0 + 3 * BIG_B + 128);
                phase<true, true>(S0, o0, vf0, S1, kf0, kf1, qb0, qb1, nmb, o1, vprev, pp);
                const bf16x8 nv0 = ldsr128(l16 + no + 1280), nv1 = ldsr128(l16 + no + 2304);
                phase<true, true>(S1, o1, vf0, S0, nk0, nk1, qa0, qa1, nma, o0, vf1, pp);
                vprev = vf1; kf0 = nk0; kf1 = nk1; vf0 = nv0; vf1 = nv1;
            }
            cur = nxt; slot = nslot;
        }
        o1 = MFMA32(vprev, __builtin_bit_cast(bf16x8, pp), o1);
        {
            float* ex = (float*)(lds + 2 * BIG_B) + wid * 12 * 64;
            bool bad = false;
#pragma unroll
            for (int r = 0; r < 12; ++r) {
                ex[r * 64 + lane] = kh ? o0[r] : o1[r]; keep[r] = kh ? o1[r] : o0[r];
                if (r < 9 || !hh) bad |= bad_f(o0[r]) | bad_f(o1[r]);
            }
            if (attempt == 0 && bad) *flag = 1;
        }
        if (attempt == 0) {
            const int wb = BIG_B + wid * TILE_B;
            const int i0 = (qb == 0) ? (8 - wid) : 0;
            float m;
            {
                const int off = wb + 8 * TILE_B;
                f32x16 s = MFMA32(ldsr128(l16 + off), qw0, f32x16{}); s = MFMA32K8(ldsr64(l8 + off), qw1, s);
#pragma unroll
                for (int r = 0; r < 16; ++r) { const int kl = (r & 3) + 8 * (r >> 2) + 4 * hh; if (kl > l31) s[r] = -3.0e38f; }
                m = fmax16(s); m = fmaxf(m, __shfl_xor(m, 32));
            }
            float mr = m + REF_OFF;
            f32x16 ob;
            for (int att = 0; att < 2; ++att) {
                if (att == 1) {
                    for (int i = i0; i < 8; ++i) {
                        const int off = wb + i * TILE_B;
                        f32x16 s = MFMA32(ldsr128(l16 + off), qw0, f32x16{}); s = MFMA32K8(ldsr64(l8 + off), qw1, s);
                        if (i == 0) {
#pragma unroll
                            for (int r = 0; r < 16; ++r) { const int kl = (r & 3) + 8 * (r >> 2) + 4 * hh; if (kl <= l31) s[r] = -3.0e38f; }
                        }
                        m = fmaxf(m, fmax16(s));
                    }
                    mr = fmaxf(m, __shfl_xor(m, 32));
                }
                const f32x16 nm = splat16(-mr);
                ob = f32x16{};
                f32x16 sa, sb;
                int off = wb + i0 * TILE_B;
                bf16x8 wv0 = ldsr128(l16 + off + 1280), wv1 = ldsr128(l16 + off + 2304), wvp = wv1;
                u32x4 wp = {0u, 0u, 0u, 0u};
                sa = MFMA32(ldsr128(l16 + off), qw0, nm); sa = MFMA32K8(ldsr64(l8 + off), qw1, sa);
                if (i0 == 0) {
#pragma unroll
                    for (int r = 0; r < 16; ++r) { const int kl = (r & 3) + 8 * (r >> 2) + 4 * hh; if (kl <= l31) sa[r] = -INFINITY; }
                }
                int i = i0;
                bool in_a = true;
                while (i < 8) {
                    {   off += TILE_B;
                        const bf16x8 nk0 = ldsr128(l16 + off), nv0 = ldsr128(l16 + off + 1280), nv1 = ldsr128(l16 + off + 2304); const bf16x4 nk1 = ldsr64(l8 + off);
                        phase<true, true>(sa, ob, wv0, sb, nk0, nk1, qw0, qw1, nm, ob, wvp, wp);
                        wvp = wv1; wv0 = nv0; wv1 = nv1; ++i; }
                    if (i == 8) { in_a = false; break; }
                    {   off += TILE_B;
                        const bf16x8 nk0 = ldsr128(l16 + off), nv0 = ldsr128(l16 + off + 1280), nv1 = ldsr128(l16 + off + 2304); const bf16x4 nk1 = ldsr64(l8 + off);
                        phase<true, true>(sb, ob, wv0, sa, nk0, nk1, qw0, qw1, nm, ob, wvp, wp);
                        wvp = wv1; wv0 = nv0; wv1 = nv1; ++i; }
                }
                if (!in_a) sa = sb;
#pragma unroll
                for (int r = 0; r < 16; ++r) { const int kl = (r & 3) + 8 * (r >> 2) + 4 * hh; if (kl > l31) sa[r] = -INFINITY; }
                phase<false, true>(sa, ob, wv0, sb, wv0, qw1, qw0, qw1, nm, ob, wvp, wp);
                ob = MFMA32(wv1, __builtin_bit_cast(bf16x8, wp), ob);
                bool bad = false;
#pragma unroll
                for (int r = 0; r < 12; ++r) if (r < 9 || !hh) bad |= bad_f(ob[r]);
                if (att == 1 || !__any(bad)) break;
            }
            store_o(ob, ws, 1, head, qb * 8 + wid, lane);
        }
        asm volatile("s_waitcnt lgkmcnt(0)\n\ts_barrier" ::: "memory");
        if (attempt == 1 || *flag == 0) {
            const float* in = (const float*)(lds + 2 * BIG_B) + (wid ^ 1) * 12 * 64;
#pragma unroll
            for (int r = 0; r < 12; ++r) keep[r] += in[r * 64 + lane];
            break;
        }
        ma = -3.0e38f; mb = -3.0e38f;
        ALLROUNDS(kvA, lds0);
        WAIT_BAR0();
        for (int c = 0; c < 4; ++c) {
            if (c < 3) { const char* nsrc = kvA + (size_t)(c + 1) * BIG_B; const unsigned nd = lds0 + ((c + 1) & 1) * BIG_B; ALLROUNDS(nsrc, nd); }
#pragma unroll 1
            for (int t = 0; t < 16; ++t) {
                const int off = (c & 1) * BIG_B + t * TILE_B;
                const bf16x8 kf0 = ldsr128(l16 + off); const bf16x4 kf1 = ldsr64(l8 + off);
                f32x16 s = MFMA32(kf0, qa0, f32x16{}); s = MFMA32K8(kf1, qa1, s);
                f32x16 u = MFMA32(kf0, qb0, f32x16{}); u = MFMA32K8(kf1, qb1, u);
                ma = fmaxf(ma, fmax16(s)); mb = fmaxf(mb, fmax16(u));
            }
            WAIT_BAR0();
        }
        ma = fmaxf(ma, __shfl_xor(ma, 32)); mb = fmaxf(mb, __shfl_xor(mb, 32));
        ALLROUNDS(kvA, lds0);
        WAIT_BAR0();
    }
    store_o(keep, ws, 0, head, qb * 8 + 2 * pr + kh, lane);
#undef ROUND
#undef ROUND6
#undef ALLROUNDS
}

constexpr int K3_CB = 2 * 160 * 160;

__global__ __launch_bounds__(768, 3) void k3_out(const char* __restrict__ ws, u16* __restrict__ out) {
    __shared__ __attribute__((aligned(16))) char lds[3 * K3_CB + 2048];
    const int tid = threadIdx.x, lane = tid & 63;
    const int wid = __builtin_amdgcn_readfirstlane(tid >> 6);
    const int bid = blockIdx.x;
    const int ft = (bid >> 3) & 7, mt = (bid & 7) * 4 + (bid >> 6);
    const int n0 = ft * 80, tt = wid & 3, br = (wid >> 2) & 1;
    const unsigned lds0 = (unsigned)(uintptr_t)lds;
    if (wid >= 8) {
        const int lw = wid - 8;
        const char* wsrc = ws + WS_WP + (size_t)ft * 4 * K3_CB;
        const unsigned vo = lw * 1024 + lane * 16;
        auto stage = [&](int ck, int buf) {
#pragma unroll
            for (int i = 0; i < 12; ++i) glds16s(vo, wsrc + (size_t)ck * K3_CB + i * 4096, lds0 + buf * K3_CB + (lw + 4 * i) * 1024);
            if (lw < 2) glds16s(vo, wsrc + (size_t)ck * K3_CB + 12 * 4096, lds0 + buf * K3_CB + (lw + 48) * 1024);
            else glds16s(vo, wsrc + (size_t)ck * K3_CB, lds0 + 3 * K3_CB + (lw - 2) * 1024);
        };
        stage(0, 0); stage(1, 1);
        asm volatile("s_waitcnt vmcnt(13)\n\ts_barrier" ::: "memory");
        stage(2, 2);
        asm volatile("s_waitcnt vmcnt(13)\n\ts_barrier" ::: "memory");
        stage(3, 0);
        asm volatile("s_waitcnt vmcnt(13)\n\ts_barrier" ::: "memory");
        asm volatile("s_waitcnt vmcnt(0)\n\ts_barrier" ::: "memory");
        return;
    }
    const int tile16 = mt * 4 + tt;
    const char* op = ws + WS_O + ((size_t)(br * 128 + tile16) * 20) * 1024 + lane * 16;
    bf16x8 of[20];
#define LDO5(b) do { const char* q_ = op + (b) * 1024; \
        asm volatile("global_load_dwordx4 %0, %5, off\n\tglobal_load_dwordx4 %1, %5, off offset:1024\n\tglobal_load_dwordx4 %2, %5, off offset:2048\n\tglobal_load_dwordx4 %3, %5, off offset:3072\n\tglobal_load_dwordx4 %4, %6, off" \
                     : "=&v"(of[(b)]), "=&v"(of[(b) + 1]), "=&v"(of[(b) + 2]), "=&v"(of[(b) + 3]), "=&v"(of[(b) + 4]) : "v"(q_), "v"(q_ + 4096) : "memory"); } while (0)
#define WAITO(str, b) asm volatile(str "\n\ts_barrier" : "+v"(of[(b)]), "+v"(of[(b) + 1]), "+v"(of[(b) + 2]), "+v"(of[(b) + 3]), "+v"(of[(b) + 4]) :: "memory")
    const int tok = tile16 * 16 + (lane & 15);
    float g;
    asm volatile("global_load_dword %0, %1, off" : "=v"(g) : "v"((const float*)(ws + WS_G) + tok) : "memory");
    LDO5(0); LDO5(5);
    const int kq = lane >> 4;
    const int woff = br * 25600 + lane * 16;
    f32x4 acc[5];
#pragma unroll
    for (int f = 0; f < 5; ++f) acc[f] = f32x4{};
    auto compute = [&](int ck) {
        const lds_cptr wb = (lds_cptr)lds + (ck % 3) * K3_CB + woff;
#pragma unroll
        for (int ks = 0; ks < 5; ++ks)
#pragma unroll
            for (int f = 0; f < 5; ++f) acc[f] = MFMA16(ldsr128(wb + ks * 5120 + f * 1024), of[ck * 5 + ks], acc[f]);
    };
    WAITO("s_waitcnt vmcnt(5)", 0);
    asm volatile("" : "+v"(g));
    LDO5(10);
    compute(0);
    WAITO("s_waitcnt vmcnt(5) lgkmcnt(0)", 5);
    LDO5(15);
    compute(1);
    WAITO("s_waitcnt vmcnt(5)", 10);
    compute(2);
    WAITO("s_waitcnt vmcnt(0) lgkmcnt(0)", 15);
    compute(3);
#undef LDO5
#undef WAITO
    float* ex = (float*)(lds + K3_CB) + tt * 20 * 64;
    if (br == 1) {
#pragma unroll
        for (int f = 0; f < 5; ++f)
#pragma unroll
            for (int r = 0; r < 4; ++r) ex[(f * 4 + r) * 64 + lane] = acc[f][r];
    }
    __syncthreads();
    if (br == 0) {
        const float g1 = rbf(1.0f - g);
        u16* orow = out + (size_t)tok * DM + n0 + kq * 4;
#pragma unroll
        for (int f = 0; f < 5; ++f) {
            float v[4];
#pragma unroll
            for (int r = 0; r < 4; ++r) v[r] = rbf(g * rbf(acc[f][r])) + rbf(g1 * rbf(ex[(f * 4 + r) * 64 + lane]));
            *(u32x2*)(orow + f * 16) = (u32x2){cvtpk(v[0], v[1]), cvtpk(v[2], v[3])};
        }
    }
}

extern "C" void kernel_launch(void* const* d_in, const int* in_sizes, int n_in, void* d_out, int out_size, void* d_ws, size_t ws_size, hipStream_t stream) {
    const u16* x = (const u16*)d_in[0];
    const u16* wa = (const u16*)d_in[1];
    const u16* woa = (const u16*)d_in[2];
    const u16* wb = (const u16*)d_in[4];
    const u16* wob = (const u16*)d_in[5];
    const u16* gw = (const u16*)d_in[6];
    char* ws = (char*)d_ws;
    k1_qkv<<<256, 768, 0, stream>>>(x, wa, wb, gw, woa, wob, ws);
    k2_attn<<<256, 512, 0, stream>>>(ws);
    k3_out<<<256, 768, 0, stream>>>(ws, (u16*)d_out);
}
```

```cpp
#include <hip/hip_runtime.h>
#include <hip/hip_bf16.h>
#include <stdint.h>

typedef __attribute__((ext_vector_type(8))) short bf16x8;
typedef __attribute__((ext_vector_type(4))) short bf16x4;
typedef __attribute__((ext_vector_type(16))) float f32x16;
typedef __attribute__((ext_vector_type(4))) float f32x4;
typedef __attribute__((ext_vector_type(4))) unsigned u32x4;
typedef __attribute__((ext_vector_type(2))) unsigned u32x2;
typedef short v4i16_t __attribute__((ext_vector_type(4)));
typedef __attribute__((address_space(3))) const char* lds_cptr;
typedef unsigned short u16;

#define DI __device__ __forceinline__
#define MFMA32(a, b, c) __builtin_amdgcn_mfma_f32_32x32x16_bf16((a), (b), (c), 0, 0, 0)
#define MFMA32K8(a, b, c) __builtin_amdgcn_mfma_f32_32x32x8bf16_1k((a), (b), (c), 0, 0, 0)
#define MFMA16(a, b, c) __builtin_amdgcn_mfma_f32_16x16x32_bf16((a), (b), (c), 0, 0, 0)

constexpr int SEQ = 2048, DM = 640, NH = 32, DH = 20;
constexpr int TILE_B = 2816;
constexpr int QT_B = 1280;
constexpr size_t WS_KV = 0;
constexpr size_t WS_Q = WS_KV + (size_t)2 * 32 * 64 * TILE_B;
constexpr size_t WS_O = WS_Q + (size_t)2 * 32 * 64 * QT_B;
constexpr size_t WS_G = WS_O + (size_t)2 * SEQ * DM * 2;
constexpr size_t WS_WP = WS_G + 8192;

typedef __attribute__((ext_vector_type(2))) __bf16 bf16x2_t;
typedef __attribute__((ext_vector_type(2))) float f32x2_t;
DI unsigned cvtpk(float lo, float hi) { const f32x2_t v = {lo, hi}; return __builtin_bit_cast(unsigned, __builtin_convertvector(v, bf16x2_t)); }
DI float bf2f(u16 v) { return __uint_as_float(((unsigned)v) << 16); }
DI float rbf(float x) { return __uint_as_float(cvtpk(x, x) << 16); }

DI void glds16s(unsigned voff, const void* sbase, unsigned lds_base) {
    unsigned sv; asm volatile("s_mov_b32 %0, m0\n\ts_mov_b32 m0, %3\n\ts_nop 0\n\tglobal_load_lds_dwordx4 %1, %2\n\ts_mov_b32 m0, %0" : "=&s"(sv) : "v"(voff), "s"(sbase), "s"(lds_base) : "memory"); }
DI const char* kernel_entry() { unsigned long long pc; asm volatile("s_getpc_b64 %0" : "=s"(pc)); return (const char*)(pc & ~255ull); }
DI void code_prefetch(const char* entry, int nkib, unsigned lds_dummy) { const int sl = blockIdx.x >> 3; if (sl < nkib) glds16s((threadIdx.x & 63) * 16, entry + sl * 1024, lds_dummy); }
DI bf16x4 trread(lds_cptr p) { return __builtin_bit_cast(bf16x4, __builtin_amdgcn_ds_read_tr16_b64_v4i16((__attribute__((address_space(3))) v4i16_t*)p)); }
DI bf16x8 ldsr128(lds_cptr p) { return *(const __attribute__((address_space(3))) bf16x8*)p; }
DI bf16x4 ldsr64(lds_cptr p) { return *(const __attribute__((address_space(3))) bf16x4*)p; }
#define WAIT_BAR0() asm volatile("s_waitcnt vmcnt(0) lgkmcnt(0)\n\ts_barrier" ::: "memory")

constexpr int K1_XB = 256 * 128;
constexpr int K1_WB = 64 * 256;
constexpr int K1_NB = 3;

struct K1Stager {
    unsigned xvo[2], wvo, lds0; const char* xbase; const char* wbase; int w, rot;
    DI void init(const u16* X, const u16* W, char* lds, int m0, int n0, int w_) {
        const int lane = threadIdx.x & 63; w = w_; lds0 = (unsigned)(uintptr_t)lds;
#pragma unroll
        for (int par = 0; par < 2; ++par) { const int r = lane >> 3; const int c = (lane & 7) ^ ((4 * par + (r >> 1)) & 7); xvo[par] = r * 1280 + c * 16; }
        wvo = (lane >> 4) * 3840 + (((lane & 15) ^ (((lane >> 4) & 3) << 2)) * 16);
        xbase = (const char*)X + (size_t)(m0 + 32 * w) * 1280;
        wbase = (const char*)W + (size_t)(8 * w) * 3840 + n0 * 2;
        rot = ((m0 >> 8) + (n0 >> 7)) % 10;
    }
    DI void stage(int kq, int buf) const {
        const int kt = (kq + rot >= 10) ? kq + rot - 10 : kq + rot;
        const unsigned xb = lds0 + buf * K1_XB + w * 4096, wb = lds0 + K1_NB * K1_XB + buf * K1_WB + w * 2048;
        const char* xs = xbase + kt * 128; const char* wsr = wbase + (size_t)kt * 64 * 3840;
#pragma unroll
        for (int j = 0; j < 2; ++j) glds16s(wvo, wsr + (size_t)j * 4 * 3840, wb + j * 1024);
#pragma unroll
        for (int i = 0; i < 4; ++i) glds16s(xvo[i & 1], xs + (size_t)i * 8 * 1280, xb + i * 1024);
    }
};
DI void k1_loader(const u16* __restrict__ X, const u16* __restrict__ W, const u16* __restrict__ Woa, const u16* __restrict__ Wob, char* __restrict__ ws, char* lds, int m0, int n0, int lw, int wgi) {
    const int lane = threadIdx.x & 63;
    K1Stager st; st.init(X, W, lds, m0, n0, lw);
    st.stage(1, 1);
    asm volatile("s_barrier" ::: "memory");
    st.stage(2, 2);
    int pb = 0;
#pragma unroll 1
    for (int kt = 1; kt < 8; ++kt) {
        asm volatile("s_waitcnt vmcnt(6)\n\ts_barrier" ::: "memory");
        st.stage(kt + 2, pb);
        pb = (pb == 2) ? 0 : pb + 1;
    }
    unsigned gv[8];
    const int t0 = (wgi * 8 + lw) * 64 + lane;
    {
        const int t = min(t0, 102399);
        const int ln = t & 63, fr = t >> 6, f = fr % 5, ks5 = (fr / 5) % 5, b = (fr / 25) & 1, ck = (fr / 50) & 3, ftile = fr / 200;
        const u16* src = (b ? Wob : Woa) + (size_t)(32 * (5 * ck + ks5) + 4 * (ln >> 4)) * DM + ftile * 80 + f * 16 + (ln & 15);
        asm volatile("global_load_ushort %0, %8, off\n\tglobal_load_ushort %1, %8, off offset:1280\n\tglobal_load_ushort %2, %8, off offset:2560\n\tglobal_load_ushort %3, %8, off offset:3840\n\t"
                     "global_load_ushort %4, %9, off\n\tglobal_load_ushort %5, %9, off offset:1280\n\tglobal_load_ushort %6, %9, off offset:2560\n\tglobal_load_ushort %7, %9, off offset:3840"
                     : "=&v"(gv[0]), "=&v"(gv[1]), "=&v"(gv[2]), "=&v"(gv[3]), "=&v"(gv[4]), "=&v"(gv[5]), "=&v"(gv[6]), "=&v"(gv[7])
                     : "v"(src), "v"(src + 16 * DM) : "memory");
    }
    asm volatile("s_waitcnt vmcnt(14)\n\ts_barrier" ::: "memory");
    asm volatile("s_waitcnt vmcnt(8)\n\ts_barrier" ::: "memory");
    asm volatile("s_waitcnt vmcnt(0)" : "+v"(gv[0]), "+v"(gv[1]), "+v"(gv[2]), "+v"(gv[3]), "+v"(gv[4]), "+v"(gv[5]), "+v"(gv[6]), "+v"(gv[7]) :: "memory");
    const u32x4 v = {gv[0] | (gv[1] << 16), gv[2] | (gv[3] << 16), gv[4] | (gv[5] << 16), gv[6] | (gv[7] << 16)};
    if (t0 < 102400) *(u32x4*)(ws + WS_WP + (size_t)t0 * 16) = v;
}

template <bool VT>
DI void k1_body(const u16* __restrict__ X, const u16* __restrict__ W, char* __restrict__ ws, char* lds, int m0, int n0, int branch, int type) {
    const int tid = threadIdx.x, lane = tid & 63, hh = lane >> 5, l31 = lane & 31;
    const int wid = __builtin_amdgcn_readfirstlane(tid >> 6);
    { K1Stager st; st.init(X, W, lds, m0, n0, wid); st.stage(0, 0); }
    const int wm = wid >> 1, wn = wid & 1;
    const int sw = (l31 >> 1) & 7;
    int xoff[4];
#pragma unroll
    for (int ks = 0; ks < 4; ++ks) xoff[ks] = (wm * 64 + l31) * 128 + (((2 * ks + hh) ^ sw) * 16);
    const int q = (lane & 15) >> 2, p = lane & 3, ch = (lane >> 4) & 1;
    int woff[2];
#pragma unroll
    for (int ni = 0; ni < 2; ++ni) woff[ni] = K1_NB * K1_XB + (8 * hh + q) * 256 + (((wn * 2 + ni) ^ q) * 64) + ch * 32 + p * 8;

    f32x16 acc[2][2];
#pragma unroll
    for (int a = 0; a < 2; ++a)
#pragma unroll
        for (int b = 0; b < 2; ++b) acc[a][b] = f32x16{};
    struct Frags { bf16x8 x0, x1, w0, w1; };
    auto ldfr = [&](int buf, int ks) {
        const lds_cptr xb = (lds_cptr)lds + buf * K1_XB + xoff[ks], wb0 = (lds_cptr)lds + buf * K1_WB + woff[0] + ks * 4096, wb1 = (lds_cptr)lds + buf * K1_WB + woff[1] + ks * 4096;
        Frags f;
        f.x0 = ldsr128(xb); f.w0 = __builtin_shufflevector(trread(wb0), trread(wb0 + 1024), 0, 1, 2, 3, 4, 5, 6, 7);
        f.w1 = __builtin_shufflevector(trread(wb1), trread(wb1 + 1024), 0, 1, 2, 3, 4, 5, 6, 7); f.x1 = ldsr128(xb + 4096);
        return f;
    };
    auto mma = [&](const Frags& f) {
        if (VT) { acc[0][0] = MFMA32(f.x0, f.w0, acc[0][0]); acc[0][1] = MFMA32(f.x0, f.w1, acc[0][1]); acc[1][0] = MFMA32(f.x1, f.w0, acc[1][0]); acc[1][1] = MFMA32(f.x1, f.w1, acc[1][1]); }
        else    { acc[0][0] = MFMA32(f.w0, f.x0, acc[0][0]); acc[0][1] = MFMA32(f.w1, f.x0, acc[0][1]); acc[1][0] = MFMA32(f.w0, f.x1, acc[1][0]); acc[1][1] = MFMA32(f.w1, f.x1, acc[1][1]); }
    };
#define K1SB() __builtin_amdgcn_sched_barrier(0)
    int buf = 0;
    Frags fa, fc;
#pragma unroll 1
    for (int kt = 0; kt < 10; ++kt) {
        asm volatile("s_waitcnt vmcnt(0) lgkmcnt(0)\n\ts_barrier" ::: "memory");
        fa = ldfr(buf, 0); K1SB();
        if (kt > 0) mma(fc); K1SB();
        fc = ldfr(buf, 1); K1SB(); mma(fa); K1SB();
        fa = ldfr(buf, 2); K1SB(); mma(fc); K1SB();
        fc = ldfr(buf, 3); K1SB(); mma(fa); K1SB();
        buf = (buf == 2) ? 0 : buf + 1;
    }
    mma(fc);
#undef K1SB
    const int fb0 = (n0 - type * 640) + wn * 64;
    if (VT) {
        char* kv = ws + WS_KV + (size_t)branch * 32 * 64 * TILE_B;
#pragma unroll
        for (int ni = 0; ni < 2; ++ni) {
            const int f = fb0 + ni * 32 + l31, head = f / 20, d = f - head * 20;
#pragma unroll
            for (int mi = 0; mi < 2; ++mi) {
                const int kt = (m0 >> 5) + wm * 2 + mi;
                char* dst = kv + ((size_t)head * 64 + kt) * TILE_B + 1280 + hh * 384 + d * 16;
#pragma unroll
                for (int s = 0; s < 2; ++s) {
                    const f32x16& a = acc[mi][ni];
                    u32x4 v = {cvtpk(a[8 * s], a[8 * s + 1]), cvtpk(a[8 * s + 2], a[8 * s + 3]), cvtpk(a[8 * s + 4], a[8 * s + 5]), cvtpk(a[8 * s + 6], a[8 * s + 7])};
                    *(u32x4*)(dst + s * 768) = v;
                    if (d >= 16) *(u32x4*)(dst + s * 768 + 64) = (d == 16) ? (u32x4){0x3f803f80u, 0x3f803f80u, 0x3f803f80u, 0x3f803f80u} : (u32x4){0u, 0u, 0u, 0u};
                }
            }
        }
    } else {
        const float sc = (type == 0) ? 0.32259857558f : 1.0f;
        char* base = (type == 0) ? (ws + WS_Q + (size_t)branch * 32 * 64 * QT_B) : (ws + WS_KV + (size_t)branch * 32 * 64 * TILE_B);
        const int rec = (type == 0) ? QT_B : TILE_B;
#pragma unroll
        for (int ni = 0; ni < 2; ++ni)
#pragma unroll
            for (int g = 0; g < 4; ++g) {
                const int f0 = fb0 + ni * 32 + 8 * g + 4 * hh, head = f0 / 20, d0 = f0 - head * 20;
                const int off = (d0 < 16) ? ((d0 >> 3) * 512 + l31 * 16 + (d0 & 7) * 2) : (1024 + l31 * 8);
#pragma unroll
                for (int mi = 0; mi < 2; ++mi) {
                    const int tt = (m0 >> 5) + wm * 2 + mi;
                    const f32x16& a = acc[mi][ni];
                    u32x2 v = {cvtpk(a[4 * g] * sc, a[4 * g + 1] * sc), cvtpk(a[4 * g + 2] * sc, a[4 * g + 3] * sc)};
                    *(u32x2*)(base + ((size_t)head * 64 + tt) * rec + off) = v;
                }
            }
    }
}

__global__ __launch_bounds__(1024, 4) void k1_qkv(const u16* __restrict__ X, const u16* __restrict__ Wa, const u16* __restrict__ Wb, const u16* __restrict__ gw,
                                                 const u16* __restrict__ Woa, const u16* __restrict__ Wob, char* __restrict__ ws) {
    __shared__ __attribute__((aligned(16))) char lds[K1_NB * (K1_XB + K1_WB) + 1024];
    const char* entry = kernel_entry();
    const int bid = blockIdx.x;
    if (__builtin_amdgcn_readfirstlane(threadIdx.x >> 6) == 15) code_prefetch(entry, 30, (unsigned)(uintptr_t)lds + K1_NB * (K1_XB + K1_WB));
    const int xcd = bid & 7, idx = bid >> 3, nq = xcd >> 1, ntl = idx >> 2;
    const int mt = (xcd & 1) * 4 + (idx & 3), ntg = nq * 8 - (nq >> 1) + ntl;
    if ((nq & 1) && ntl == 7) {
        if (threadIdx.x >= 512) { asm volatile("s_waitcnt vmcnt(0)" ::: "memory"); return; }
        const int tok = (((nq >> 1) * 2 + (xcd & 1)) * 4 + (idx & 3)) * 128 + (threadIdx.x >> 2), part = threadIdx.x & 3;
        const u16* xr = X + (size_t)tok * DM + part * 160; const u16* gr = gw + part * 160;
        float s = 0.f;
#pragma unroll 4
        for (int i = 0; i < 20; ++i) {
            const bf16x8 xv = *(const bf16x8*)(xr + i * 8), gv = *(const bf16x8*)(gr + i * 8);
#pragma unroll
            for (int j = 0; j < 8; ++j) s += rbf(bf2f((u16)xv[j]) * bf2f((u16)gv[j]));
        }
        s += __shfl_xor(s, 1); s += __shfl_xor(s, 2);
        if (part == 0) { const float z = rbf(s); ((float*)(ws + WS_G))[tok] = rbf(1.0f / (1.0f + __expf(-z))); }
        return;
    }
    const int branch = ntg / 15, nt = ntg - branch * 15, type = nt / 5;
    const u16* W = branch ? Wb : Wa;
    const int wv = __builtin_amdgcn_readfirstlane(threadIdx.x >> 6);
    if (wv >= 8) { k1_loader(X, W, Woa, Wob, ws, lds, mt * 256, nt * 128, wv - 8, ntg * 8 + mt); return; }
    if (type == 2) k1_body<true>(X, W, ws, lds, mt * 256, nt * 128, branch, type);
    else k1_body<false>(X, W, ws, lds, mt * 256, nt * 128, branch, type);
}

constexpr int BIG_B = 16 * TILE_B;
constexpr int K2_LDS = 3 * BIG_B + 128 + 1024;
constexpr float REF_OFF = 96.0f;
#define SB() __builtin_amdgcn_sched_barrier(0)
#define PIN(x) asm volatile("" : "+v"(x))
#define EX(v) __builtin_amdgcn_exp2f(v)

DI float fmax16(const f32x16& s) {
    float a = fmaxf(fmaxf(s[0], s[1]), s[2]), b = fmaxf(fmaxf(s[3], s[4]), s[5]);
    a = fmaxf(fmaxf(a, s[6]), s[7]); b = fmaxf(fmaxf(b, s[8]), s[9]);
    a = fmaxf(fmaxf(a, s[10]), s[11]); b = fmaxf(fmaxf(b, s[12]), s[13]);
    a = fmaxf(fmaxf(a, s[14]), s[15]);
    return fmaxf(a, b);
}
DI f32x16 splat16(float v) { f32x16 r;
#pragma unroll
    for (int i = 0; i < 16; ++i) r[i] = v; return r; }
template <bool NEXT, bool PREV>
DI void phase(f32x16& X, f32x16& o, const bf16x8& vf0, f32x16& Y, const bf16x8& kf0, const bf16x4& kf1, const bf16x8& qy0, const bf16x4& qy1, const f32x16& nmy,
              f32x16& op, const bf16x8& vp, u32x4& pp) {
    SB();
    X[0] = EX(X[0]); X[1] = EX(X[1]); PIN(X); SB();
    if (PREV) { op = MFMA32(vp, __builtin_bit_cast(bf16x8, pp), op); PIN(op); SB(); }
    X[2] = EX(X[2]); X[3] = EX(X[3]); X[4] = EX(X[4]); X[5] = EX(X[5]); X[6] = EX(X[6]); PIN(X); SB();
    if (NEXT) { Y = MFMA32(kf0, qy0, nmy); PIN(Y); SB(); }
    X[7] = EX(X[7]); PIN(X); SB();
    u32x4 p0 = {cvtpk(X[0], X[1]), cvtpk(X[2], X[3]), cvtpk(X[4], X[5]), cvtpk(X[6], X[7])}; PIN(p0); SB();
    X[8] = EX(X[8]); PIN(X); SB();
    if (NEXT) { Y = MFMA32K8(kf1, qy1, Y); PIN(Y); SB(); }
    X[9] = EX(X[9]); X[10] = EX(X[10]); X[11] = EX(X[11]); X[12] = EX(X[12]); PIN(X); SB();
    o = MFMA32(vf0, __builtin_bit_cast(bf16x8, p0), o); PIN(o); SB();
    X[13] = EX(X[13]); X[14] = EX(X[14]); X[15] = EX(X[15]); PIN(X); SB();
    pp = (u32x4){cvtpk(X[8], X[9]), cvtpk(X[10], X[11]), cvtpk(X[12], X[13]), cvtpk(X[14], X[15])}; PIN(pp); SB();
}
DI bool bad_f(float x) { return !(fabsf(x) <= 1.0e37f); }
DI void store_o(const f32x16& o, char* ws, int branch, int head, int qtile, int lane) {
    const int hh = lane >> 5, l31 = lane & 31;
    float l = o[8];
    l = __shfl(l, 32 + l31);
    const float rl = __builtin_amdgcn_rcpf(l);
    const int tok = qtile * 32 + l31;
    char* base = ws + WS_O + ((size_t)(branch * 128 + (tok >> 4)) * 20) * 1024 + (tok & 15) * 16;
#pragma unroll
    for (int g = 0; g < 3; ++g) {
        if (g == 2 && hh) break;
        const int k0 = head * 20 + 8 * g + 4 * hh;
        const int ks = k0 >> 5, kk = k0 & 31;
        u32x2 v = {cvtpk(o[4 * g] * rl, o[4 * g + 1] * rl), cvtpk(o[4 * g + 2] * rl, o[4 * g + 3] * rl)};
        *(u32x2*)(base + ks * 1024 + ((kk & 15) >> 2) * 256 + (kk >> 4) * 8) = v;
    }
}

__global__ __launch_bounds__(512, 2) void k2_attn(char* __restrict__ ws) {
    __shared__ __attribute__((aligned(16))) char lds[K2_LDS];
    const char* entry = kernel_entry();
    const int tid = threadIdx.x, lane = tid & 63, hh = lane >> 5, l31 = lane & 31;
    const int wid = __builtin_amdgcn_readfirstlane(tid >> 6);
    const int bid = blockIdx.x;
    const int head = (bid & 7) * 4 + ((bid >> 3) & 3), qb = bid >> 5;
    const unsigned lds0 = (unsigned)(uintptr_t)lds;
    const char* kvA = ws + WS_KV + (size_t)head * 64 * TILE_B;
    const char* kvW = ws + WS_KV + ((size_t)(32 + head) * 64 + qb * 8 - 8) * TILE_B;
    volatile __attribute__((address_space(3))) int* flag = (volatile __attribute__((address_space(3))) int*)((__attribute__((address_space(3))) char*)lds + 3 * BIG_B);
    const int pr = wid >> 1, kh = wid & 1;
    const char* wpf = ws + WS_WP + (size_t)(bid >> 3) * 51200;
    const lds_cptr l16 = (lds_cptr)lds + lane * 16, l8 = (lds_cptr)lds + (hh ? 1280 + 21 * 16 : 1024 + l31 * 8);
    const lds_cptr lv = (lds_cptr)lds + 1280 + hh * 384 + min(l31, 23) * 16;
    const unsigned vo16 = wid * 1024 + lane * 16;
#define ROUND(src, slotbase, r) glds16s(vo16, (src) + (r) * 8192, (slotbase) + wid * 1024 + (r) * 8192)
#define ROUND6(src, slotbase) do { if (wid < 4) ROUND(src, slotbase, 5); } while (0)
#define ALLROUNDS(src, slotbase) do { ROUND(src, slotbase, 0); ROUND(src, slotbase, 1); ROUND(src, slotbase, 2); ROUND(src, slotbase, 3); ROUND(src, slotbase, 4); ROUND6(src, slotbase); } while (0)
    if (wid == 7) code_prefetch(entry, 20, lds0 + 3 * BIG_B + 128);
    ALLROUNDS(kvA, lds0);
    bf16x8 qa0, qb0, qw0; bf16x4 qa1, qb1, qw1;
    {
        const char* qp = ws + WS_Q + ((size_t)head * 64 + qb * 8 + 2 * pr) * QT_B;
        const char* qq = ws + WS_Q + ((size_t)(32 + head) * 64 + qb * 8 + wid) * QT_B;
        qa0 = *(const bf16x8*)(qp + lane * 16); qb0 = *(const bf16x8*)(qp + QT_B + lane * 16); qw0 = *(const bf16x8*)(qq + lane * 16);
        const bf16x4 ta = *(const bf16x4*)(qp + 1024 + l31 * 8), tb = *(const bf16x4*)(qp + QT_B + 1024 + l31 * 8), tw = *(const bf16x4*)(qq + 1024 + l31 * 8);
        qa1 = hh ? bf16x4{0, 0, 0, 0} : ta; qb1 = hh ? bf16x4{0, 0, 0, 0} : tb; qw1 = hh ? bf16x4{0, 0, 0, 0} : tw;
    }
    if (tid == 0) *flag = 0;
    WAIT_BAR0();
    float ma, mb;
    {
        const bf16x8 kf0 = ldsr128(l16); const bf16x4 kf1 = ldsr64(l8);
        f32x16 s = MFMA32(kf0, qa0, f32x16{}); s = MFMA32K8(kf1, qa1, s);
        f32x16 u = MFMA32(kf0, qb0, f32x16{}); u = MFMA32K8(kf1, qb1, u);
        ma = fmax16(s); ma = fmaxf(ma, __shfl_xor(ma, 32)) + REF_OFF;
        mb = fmax16(u); mb = fmaxf(mb, __shfl_xor(mb, 32)) + REF_OFF;
    }
    f32x16 keep;
    for (int attempt = 0;; ++attempt) {
        const f32x16 nma = splat16(-ma), nmb = splat16(-mb);
        f32x16 o0 = f32x16{}, o1 = f32x16{}, S0, S1;
        int cur = kh * 8 * TILE_B;
        bf16x8 kf0 = ldsr128(l16 + cur), vf0 = ldsr128(lv + cur), vf1 = ldsr128(lv + cur + 768); bf16x4 kf1 = ldsr64(l8 + cur);
        S0 = MFMA32(kf0, qa0, nma); S0 = MFMA32K8(kf1, qa1, S0);
        bf16x8 vprev = vf1; u32x4 pp = {0u, 0u, 0u, 0u};
        int slot = 0;
#pragma unroll 1
        for (int c = 0; c < 4; ++c) {
            const int nslot = (slot == 2) ? 0 : slot + 1;
            const int nxt = nslot * BIG_B + kh * 8 * TILE_B;
            const char* dsrc = (c < 3) ? kvA + (size_t)(c + 1) * BIG_B : kvW;
            const unsigned ddst = lds0 + nslot * BIG_B;
            const char* wpre = wpf + (c & 1) * 24576 + (c >> 1) * 2048;
#pragma unroll
            for (int t = 0; t < 8; ++t) {
                if (t == 7) asm volatile("s_waitcnt vmcnt(0)\n\ts_barrier" ::: "memory");
                const int no = (t < 7) ? cur + (t + 1) * TILE_B : nxt;
                const bf16x8 nk0 = ldsr128(l16 + no); const bf16x4 nk1 = ldsr64(l8 + no);
                if (t < 2) { ROUND(dsrc, ddst, 2 * t); ROUND(dsrc, ddst, 2 * t + 1); }
                if (t == 2) { ROUND(dsrc, ddst, 4); ROUND6(dsrc, ddst); }
                if (t >= 3 && t < 6) glds16s(vo16, wpre + (t - 3) * 8192, lds0 + 3 * BIG_B + 128);
                phase<true, true>(S0, o0, vf0, S1, kf0, kf1, qb0, qb1, nmb, o1, vprev, pp);
                const bf16x8 nv0 = ldsr128(lv + no), nv1 = ldsr128(lv + no + 768);
                phase<true, true>(S1, o1, vf0, S0, nk0, nk1, qa0, qa1, nma, o0, vf1, pp);
                vprev = vf1; kf0 = nk0; kf1 = nk1; vf0 = nv0; vf1 = nv1;
            }
            cur = nxt; slot = nslot;
        }
        o1 = MFMA32(vprev, __builtin_bit_cast(bf16x8, pp), o1);
        {
            float* ex = (float*)(lds + 2 * BIG_B) + wid * 12 * 64;
            bool bad = false;
#pragma unroll
            for (int r = 0; r < 12; ++r) {
                ex[r * 64 + lane] = kh ? o0[r] : o1[r]; keep[r] = kh ? o1[r] : o0[r];
                if (r < 9 || !hh) bad |= bad_f(o0[r]) | bad_f(o1[r]);
            }
            if (attempt == 0 && bad) *flag = 1;
        }
        if (attempt == 0) {
            const int wb = BIG_B + wid * TILE_B;
            const int i0 = (qb == 0) ? (8 - wid) : 0, n = 9 - i0;
            float mr = 0.f;
            f32x16 ob;
            for (int att = 0; att < 2; ++att) {
                if (att == 1) {
                    float m = mr - REF_OFF;
                    for (int i = i0; i < 8; ++i) {
                        const int off = wb + i * TILE_B;
                        f32x16 s = MFMA32(ldsr128(l16 + off), qw0, f32x16{}); s = MFMA32K8(ldsr64(l8 + off), qw1, s);
                        if (i == 0) {
#pragma unroll
                            for (int r = 0; r < 16; ++r) { const int kl = (r & 3) + 8 * (r >> 2) + 4 * hh; if (kl <= l31) s[r] = -3.0e38f; }
                        }
                        m = fmaxf(m, fmax16(s));
                    }
                    mr = fmaxf(m, __shfl_xor(m, 32));
                }
                f32x16 sa, sb;
                const int offd = wb + 8 * TILE_B;
                int off = wb + i0 * TILE_B;
                sa = MFMA32(ldsr128(l16 + offd), qw0, f32x16{}); sa = MFMA32K8(ldsr64(l8 + offd), qw1, sa);
                bf16x8 wv0 = ldsr128(lv + offd), wv1 = ldsr128(lv + offd + 768), wvp = wv1;
                bf16x8 wk0 = ldsr128(l16 + off); bf16x4 wk1 = ldsr64(l8 + off);
                u32x4 wp = {0u, 0u, 0u, 0u};
#pragma unroll
                for (int r = 0; r < 16; ++r) { const int kl = (r & 3) + 8 * (r >> 2) + 4 * hh; if (kl > l31) sa[r] = -INFINITY; }
                if (att == 0) { float m = fmax16(sa); mr = fmaxf(m, __shfl_xor(m, 32)) + REF_OFF; }
#pragma unroll
                for (int r = 0; r < 16; ++r) sa[r] -= mr;
                const f32x16 nm = splat16(-mr);
                ob = f32x16{};
                int j = 1;
                bool in_a = true;
                if (n == 9) {
#pragma unroll
                    for (int jj = 0; jj < 4; ++jj) {
                        {   const int o2 = wb + 2 * jj * TILE_B;
                            const bf16x8 nk0 = ldsr128(l16 + o2 + TILE_B), nv0 = ldsr128(lv + o2), nv1 = ldsr128(lv + o2 + 768); const bf16x4 nk1 = ldsr64(l8 + o2 + TILE_B);
                            phase<true, true>(sa, ob, wv0, sb, wk0, wk1, qw0, qw1, nm, ob, wvp, wp);
                            if (jj == 0) {
#pragma unroll
                                for (int r = 0; r < 16; ++r) { const int kl = (r & 3) + 8 * (r >> 2) + 4 * hh; if (kl <= l31) sb[r] = -INFINITY; }
                            }
                            wvp = wv1; wv0 = nv0; wv1 = nv1; wk0 = nk0; wk1 = nk1; }
                        {   const int o2 = wb + (2 * jj + 1) * TILE_B;
                            const bf16x8 nk0 = ldsr128(l16 + o2 + TILE_B), nv0 = ldsr128(lv + o2), nv1 = ldsr128(lv + o2 + 768); const bf16x4 nk1 = ldsr64(l8 + o2 + TILE_B);
                            phase<true, true>(sb, ob, wv0, sa, wk0, wk1, qw0, qw1, nm, ob, wvp, wp);
                            wvp = wv1; wv0 = nv0; wv1 = nv1; wk0 = nk0; wk1 = nk1; }
                    }
                    j = 9;
                }
                while (j < n) {
                    {   const bf16x8 nk0 = ldsr128(l16 + off + TILE_B), nv0 = ldsr128(lv + off), nv1 = ldsr128(lv + off + 768); const bf16x4 nk1 = ldsr64(l8 + off + TILE_B);
                        phase<true, true>(sa, ob, wv0, sb, wk0, wk1, qw0, qw1, nm, ob, wvp, wp);
                        if (j == 1 && i0 == 0) {
#pragma unroll
                            for (int r = 0; r < 16; ++r) { const int kl = (r & 3) + 8 * (r >> 2) + 4 * hh; if (kl <= l31) sb[r] = -INFINITY; }
                        }
                        wvp = wv1; wv0 = nv0; wv1 = nv1; wk0 = nk0; wk1 = nk1; off += TILE_B; ++j; }
                    if (j == n) { in_a = false; break; }
                    {   const bf16x8 nk0 = ldsr128(l16 + off + TILE_B), nv0 = ldsr128(lv + off), nv1 = ldsr128(lv + off + 768); const bf16x4 nk1 = ldsr64(l8 + off + TILE_B);
                        phase<true, true>(sb, ob, wv0, sa, wk0, wk1, qw0, qw1, nm, ob, wvp, wp);
                        wvp = wv1; wv0 = nv0; wv1 = nv1; wk0 = nk0; wk1 = nk1; off += TILE_B; ++j; }
                }
                if (!in_a) sa = sb;
                phase<false, true>(sa, ob, wv0, sb, wv0, qw1, qw0, qw1, nm, ob, wvp, wp);
                ob = MFMA32(wv1, __builtin_bit_cast(bf16x8, wp), ob);
                bool bad = false;
#pragma unroll
                for (int r = 0; r < 12; ++r) if (r < 9 || !hh) bad |= bad_f(ob[r]);
                if (att == 1 || !__any(bad)) break;
            }
            store_o(ob, ws, 1, head, qb * 8 + wid, lane);
        }
        asm volatile("s_waitcnt lgkmcnt(0)\n\ts_barrier" ::: "memory");
        if (attempt == 1 || *flag == 0) {
            const float* in = (const float*)(lds + 2 * BIG_B) + (wid ^ 1) * 12 * 64;
#pragma unroll
            for (int r = 0; r < 12; ++r) keep[r] += in[r * 64 + lane];
            break;
        }
        ma = -3.0e38f; mb = -3.0e38f;
        ALLROUNDS(kvA, lds0);
        WAIT_BAR0();
        for (int c = 0; c < 4; ++c) {
            if (c < 3) { const char* nsrc = kvA + (size_t)(c + 1) * BIG_B; const unsigned nd = lds0 + ((c + 1) & 1) * BIG_B; ALLROUNDS(nsrc, nd); }
#pragma unroll 1
            for (int t = 0; t < 16; ++t) {
                const int off = (c & 1) * BIG_B + t * TILE_B;
                const bf16x8 kf0 = ldsr128(l16 + off); const bf16x4 kf1 = ldsr64(l8 + off);
                f32x16 s = MFMA32(kf0, qa0, f32x16{}); s = MFMA32K8(kf1, qa1, s);
                f32x16 u = MFMA32(kf0, qb0, f32x16{}); u = MFMA32K8(kf1, qb1, u);
                ma = fmaxf(ma, fmax16(s)); mb = fmaxf(mb, fmax16(u));
            }
            WAIT_BAR0();
        }
        ma = fmaxf(ma, __shfl_xor(ma, 32)); mb = fmaxf(mb, __shfl_xor(mb, 32));
        ALLROUNDS(kvA, lds0);
        WAIT_BAR0();
    }
    store_o(keep, ws, 0, head, qb * 8 + 2 * pr + kh, lane);
#undef ROUND
#undef ROUND6
#undef ALLROUNDS
}

constexpr int K3_CB = 2 * 160 * 160;

__global__ __launch_bounds__(768, 3) void k3_out(const char* __restrict__ ws, u16* __restrict__ out) {
    __shared__ __attribute__((aligned(16))) char lds[3 * K3_CB + 2048];
    const char* entry = kernel_entry();
    const int tid = threadIdx.x, lane = tid & 63;
    const int wid = __builtin_amdgcn_readfirstlane(tid >> 6);
    const int bid = blockIdx.x;
    const int ft = (bid >> 3) & 7, mt = (bid & 7) * 4 + (bid >> 6);
    const int n0 = ft * 80, tt = wid & 3, br = (wid >> 2) & 1;
    const unsigned lds0 = (unsigned)(uintptr_t)lds;
    if (wid >= 8) {
        const int lw = wid - 8;
        if (lw == 3) code_prefetch(entry, 6, lds0 + 3 * K3_CB + 1024);
        const char* wsrc = ws + WS_WP + (size_t)ft * 4 * K3_CB;
        const unsigned vo = lw * 1024 + lane * 16;
        auto stage = [&](int ck, int buf) {
#pragma unroll
            for (int i = 0; i < 12; ++i) glds16s(vo, wsrc + (size_t)ck * K3_CB + i * 4096, lds0 + buf * K3_CB + (lw + 4 * i) * 1024);
            if (lw < 2) glds16s(vo, wsrc + (size_t)ck * K3_CB + 12 * 4096, lds0 + buf * K3_CB + (lw + 48) * 1024);
            else glds16s(vo, wsrc + (size_t)ck * K3_CB, lds0 + 3 * K3_CB + (lw - 2) * 1024);
        };
        stage(0, 0); stage(1, 1);
        asm volatile("s_waitcnt vmcnt(13)\n\ts_barrier" ::: "memory");
        stage(2, 2);
        asm volatile("s_waitcnt vmcnt(13)\n\ts_barrier" ::: "memory");
        stage(3, 0);
        asm volatile("s_waitcnt vmcnt(13)\n\ts_barrier" ::: "memory");
        asm volatile("s_waitcnt vmcnt(0)\n\ts_barrier" ::: "memory");
        return;
    }
    const int tile16 = mt * 4 + tt;
    const char* op = ws + WS_O + ((size_t)(br * 128 + tile16) * 20) * 1024 + lane * 16;
    bf16x8 of[20];
#define LDO5(b) do { const char* q_ = op + (b) * 1024; \
        asm volatile("global_load_dwordx4 %0, %5, off\n\tglobal_load_dwordx4 %1, %5, off offset:1024\n\tglobal_load_dwordx4 %2, %5, off offset:2048\n\tglobal_load_dwordx4 %3, %5, off offset:3072\n\tglobal_load_dwordx4 %4, %6, off" \
                     : "=&v"(of[(b)]), "=&v"(of[(b) + 1]), "=&v"(of[(b) + 2]), "=&v"(of[(b) + 3]), "=&v"(of[(b) + 4]) : "v"(q_), "v"(q_ + 4096) : "memory"); } while (0)
#define WAITO(str, b) asm volatile(str "\n\ts_barrier" : "+v"(of[(b)]), "+v"(of[(b) + 1]), "+v"(of[(b) + 2]), "+v"(of[(b) + 3]), "+v"(of[(b) + 4]) :: "memory")
    const int tok = tile16 * 16 + (lane & 15);
    float g;
    asm volatile("global_load_dword %0, %1, off" : "=v"(g) : "v"((const float*)(ws + WS_G) + tok) : "memory");
    LDO5(0); LDO5(5);
    const int kq = lane >> 4;
    const int woff = br * 25600 + lane * 16;
    f32x4 acc[5];
#pragma unroll
    for (int f = 0; f < 5; ++f) acc[f] = f32x4{};
    auto compute = [&](int ck) {
        const lds_cptr wb = (lds_cptr)lds + (ck % 3) * K3_CB + woff;
#pragma unroll
        for (int ks = 0; ks < 5; ++ks)
#pragma unroll
            for (int f = 0; f < 5; ++f) acc[f] = MFMA16(ldsr128(wb + ks * 5120 + f * 1024), of[ck * 5 + ks], acc[f]);
    };
    WAITO("s_waitcnt vmcnt(5)", 0);
    asm volatile("" : "+v"(g));
    LDO5(10);
    compute(0);
    WAITO("s_waitcnt vmcnt(5) lgkmcnt(0)", 5);
    LDO5(15);
    compute(1);
    WAITO("s_waitcnt vmcnt(5)", 10);
    compute(2);
    WAITO("s_waitcnt vmcnt(0) lgkmcnt(0)", 15);
    compute(3);
#undef LDO5
#undef WAITO
    float* ex = (float*)(lds + K3_CB) + tt * 20 * 64;
    if (br == 1) {
#pragma unroll
        for (int f = 0; f < 5; ++f)
#pragma unroll
            for (int r = 0; r < 4; ++r) ex[(f * 4 + r) * 64 + lane] = acc[f][r];
    }
    __syncthreads();
    if (br == 0) {
        const float g1 = rbf(1.0f - g);
        u16* orow = out + (size_t)tok * DM + n0 + kq * 4;
#pragma unroll
        for (int f = 0; f < 5; ++f) {
            float v[4];
#pragma unroll
            for (int r = 0; r < 4; ++r) v[r] = rbf(g * rbf(acc[f][r])) + rbf(g1 * rbf(ex[(f * 4 + r) * 64 + lane]));
            *(u32x2*)(orow + f * 16) = (u32x2){cvtpk(v[0], v[1]), cvtpk(v[2], v[3])};
        }
    }
}

extern "C" void kernel_launch(void* const* d_in, const int* in_sizes, int n_in, void* d_out, int out_size, void* d_ws, size_t ws_size, hipStream_t stream) {
    const u16* x = (const u16*)d_in[0];
    const u16* wa = (const u16*)d_in[1];
    const u16* woa = (const u16*)d_in[2];
    const u16* wb = (const u16*)d_in[4];
    const u16* wob = (const u16*)d_in[5];
    const u16* gw = (const u16*)d_in[6];
    char* ws = (char*)d_ws;
    k1_qkv<<<256, 1024, 0, stream>>>(x, wa, wb, gw, woa, wob, ws);
    k2_attn<<<256, 512, 0, stream>>>(ws);
    k3_out<<<256, 768, 0, stream>>>(ws, (u16*)d_out);
}
```
